```python
import torch

def _softmax(x, axis=-1):
    return torch.softmax(x, dim=axis)
def _gelu(x):
    return torch.nn.functional.gelu(x, approximate="tanh")
def _silu(x):
    return torch.nn.functional.silu(x)
def _sigmoid(x):
    return torch.sigmoid(x)
def _layernorm(x, axis=-1, eps=1e-5):
    m = x.mean(dim=axis, keepdim=True)
    v = x.var(dim=axis, keepdim=True, unbiased=False)
    return (x - m) / torch.sqrt(v + eps)
def _scatter_add(tgt, idx, src, axis=0):
    return tgt.clone().index_add_(axis, idx, src)
def _segment_sum(src, seg, n):
    out = torch.zeros((n,) + src.shape[1:], dtype=src.dtype)
    return out.index_add_(0, seg, src)
def _pad(x, pw):
    flat = tuple(v for pair in reversed(pw) for v in pair)
    return torch.nn.functional.pad(x, flat)

kernel_name = 'sequential_preLN__mha_block__geglu_ffn__prompt'

def setup_inputs():

    _w = (lambda a: (lambda t: t.to(torch.bfloat16) if t.is_floating_point() else t)(torch.as_tensor(a)))
    def _A_setup():
        B, S, D, H = 1, 2048, 512, 32
        return {
            "x": _w((lambda *s: torch.randn(*s, dtype=torch.bfloat16) * 0.05)(B, S, D)),
            "wqkv": _w((lambda *s: torch.randn(*s, dtype=torch.bfloat16) * 0.05)(D, 3 * D)),
            "wo": _w((lambda *s: torch.randn(*s, dtype=torch.bfloat16) * 0.05)(D, D)),
        }
    _A_in = _A_setup()

    def _B_setup():
        B, S, D, F = 1, 2048, 512, 1792
        return {
            "x": _w((lambda *s: torch.randn(*s, dtype=torch.bfloat16) * 0.05)(B, S, D)),
            "w_gate": _w((lambda *s: torch.randn(*s, dtype=torch.bfloat16) * 0.05)(D, F)),
            "w_up": _w((lambda *s: torch.randn(*s, dtype=torch.bfloat16) * 0.05)(D, F)),
            "w_down": _w((lambda *s: torch.randn(*s, dtype=torch.bfloat16) * 0.05)(F, D)),
        }
    _B_in = _B_setup()

    return {
        "A_x": _A_in["x"],
        "A_wqkv": _A_in["wqkv"],
        "A_wo": _A_in["wo"],
        "B_x": _B_in["x"],
        "B_w_gate": _B_in["w_gate"],
        "B_w_up": _B_in["w_up"],
        "B_w_down": _B_in["w_down"],
    }

def reference(A_x, A_wqkv, A_wo, B_x, B_w_gate, B_w_up, B_w_down):
    def _A_ref(x, wqkv, wo):
        B, S, D = x.shape
        H = 32
        Dh = D // H
        qkv = (lambda x, *d: x.permute(*d))((x @ wqkv).reshape(B, S, 3, H, Dh), 2, 0, 3, 1, 4)
        q, k, v = qkv[0], qkv[1], qkv[2]
        scores = torch.einsum("bhqd,bhkd->bhqk", q, k) * (Dh ** -0.5)
        p = _softmax(scores, axis=-1)
        o = torch.einsum("bhqk,bhkd->bhqd", p, v)
        return (lambda x, *d: x.permute(*d))(o, 0, 2, 1, 3).reshape(B, S, D) @ wo

    def _B_ref(x, w_gate, w_up, w_down):
        return (_gelu(x @ w_gate) * (x @ w_up)) @ w_down

    x = A_x
    h = x + _A_ref(_layernorm(x, axis=-1), A_wqkv, A_wo)
    h2 = h + _B_ref(_layernorm(h, axis=-1), B_w_gate, B_w_up, B_w_down)
    return h2
```

```cpp
#include <hip/hip_runtime.h>
#include <hip/hip_bf16.h>
#include <stdint.h>

typedef __attribute__((ext_vector_type(8))) short bf16x8;
typedef __attribute__((ext_vector_type(4))) short s16x4;
typedef __attribute__((ext_vector_type(16))) float f32x16;
typedef __attribute__((ext_vector_type(4))) float f32x4;
typedef __attribute__((ext_vector_type(2))) float f32x2;
typedef __attribute__((ext_vector_type(4))) unsigned u32x4;
typedef __attribute__((ext_vector_type(2))) unsigned u32x2;
typedef __attribute__((ext_vector_type(2))) __bf16 bf16x2;
#define LAS __attribute__((address_space(3)))
typedef LAS char* lptr;

#define MFMA(a, b, c) __builtin_amdgcn_mfma_f32_32x32x16_bf16(a, b, c, 0, 0, 0)
#define SBAR() __builtin_amdgcn_sched_barrier(0)
#define WAITBAR(N) asm volatile("s_waitcnt vmcnt(" #N ") lgkmcnt(0)\n\ts_barrier" ::: "memory")
#define LBAR() asm volatile("s_waitcnt lgkmcnt(0)\n\ts_barrier" ::: "memory")

constexpr int S = 2048, D = 512, H = 32, F = 1792;
constexpr size_t WS_Q = 0;
constexpr size_t WS_K = WS_Q + (2u << 20);
constexpr size_t WS_V = WS_K + (2u << 20);
constexpr size_t WS_O = WS_V + (2u << 20);
constexpr size_t WS_H = WS_O + (2u << 20);
constexpr size_t WS_ACT = WS_H + (2u << 20);
constexpr size_t WS_WO = WS_ACT + (7u << 20);
constexpr size_t WS_WGU = WS_WO + (512u << 10);
constexpr size_t WS_WD = WS_WGU + (3584u << 10);

__device__ __forceinline__ void glds(const void* sbase, unsigned voff, unsigned ldsaddr) {
    asm volatile("s_mov_b32 m0, %2\n\ts_nop 0\n\tglobal_load_lds_dwordx4 %0, %1" :: "v"(voff), "s"(sbase), "s"(ldsaddr) : "memory");
}
__device__ __forceinline__ bf16x8 lds128(lptr p) { return *(const LAS bf16x8*)p; }
__device__ __forceinline__ s16x4 ldstr(lptr p) { return __builtin_amdgcn_ds_read_tr16_b64_v4i16((LAS s16x4*)p); }
__device__ __forceinline__ bf16x8 cat(s16x4 a, s16x4 b) { return __builtin_shufflevector(a, b, 0, 1, 2, 3, 4, 5, 6, 7); }
__device__ __forceinline__ unsigned pk(float lo, float hi) {
    bf16x2 r = {(__bf16)lo, (__bf16)hi};
    return __builtin_bit_cast(unsigned, r);
}
__device__ __forceinline__ float dot2(unsigned a, unsigned b, float c) {
    return __builtin_amdgcn_fdot2_f32_bf16(__builtin_bit_cast(bf16x2, a), __builtin_bit_cast(bf16x2, b), c, false);
}
__device__ __forceinline__ float bflo(unsigned u) { return __uint_as_float(u << 16); }
__device__ __forceinline__ float bfhi(unsigned u) { return __uint_as_float(u & 0xffff0000u); }
constexpr unsigned ONES2 = 0x3F803F80u;
__device__ __forceinline__ void stat2(bf16x8 f, float& s1, float& s2) {
    u32x4 u = __builtin_bit_cast(u32x4, f);
#pragma unroll
    for (int i = 0; i < 4; ++i) { s1 = dot2(u[i], ONES2, s1); s2 = dot2(u[i], u[i], s2); }
}
__device__ __forceinline__ void stat1(bf16x8 f, float& s1) {
    u32x4 u = __builtin_bit_cast(u32x4, f);
#pragma unroll
    for (int i = 0; i < 4; ++i) s1 = dot2(u[i], ONES2, s1);
}
__device__ __forceinline__ int rfl(int x) { return __builtin_amdgcn_readfirstlane(x); }

constexpr int K1_SLOT = 32768;
template <int NU, bool SWAP>
__device__ __forceinline__ void k1_loop(lptr lds, const char* xs, const char* ws, unsigned xoff, unsigned woff, unsigned ldsw, int start,
                                        int lane, int t, int seg, int hl0, int u, f32x16 (&acc)[2], float& s1, float& s2, float (&cs)[2]) {
    const int r = lane & 31, h = lane >> 5;
    const int row = 32 * t + r;
    const unsigned xa = row * 128 + ((h ^ ((row >> 1) & 7)) << 4);
    const int q = (lane & 15) >> 2, p = lane & 3, blk = (lane >> 4) & 1;
    unsigned wa[2];
#pragma unroll
    for (int i = 0; i < 2; ++i)
        wa[i] = 8192 + seg * 8192 + (8 * h + q) * 128 + ((((hl0 + i) * 4 + blk * 2 + (p >> 1)) ^ (((q >> 1) & 1) << 2)) << 4) + (p & 1) * 8;
    auto issue = [&](int ch) {
        const int k = (ch + start) & 7;
        const unsigned l = ldsw + (ch & 3) * K1_SLOT;
        glds(xs + k * 128, xoff, l);
        const char* w = ws + (size_t)k * 64 * 3072;
        glds(w, woff, l + 8192); glds(w + 1024, woff, l + 16384); glds(w + 2048, woff, l + 24576);
    };
    issue(0); issue(1); issue(2); issue(3);
#pragma unroll
    for (int ch = 0; ch < 8; ++ch) {
        if (ch == 0) WAITBAR(12); else if (ch < 6) WAITBAR(8); else if (ch == 6) WAITBAR(4); else WAITBAR(0);
        if (ch >= 1 && ch + 3 < 8) issue(ch + 3);
        lptr sl = lds + (ch & 3) * K1_SLOT;
#pragma unroll
        for (int ks = 0; ks < 4; ++ks) {
            bf16x8 xf = lds128(sl + (xa ^ (ks << 5)));
            if (ks == u) stat2(xf, s1, s2);
#pragma unroll
            for (int i = 0; i < NU; ++i) {
                bf16x8 wf = cat(ldstr(sl + wa[i] + ks * 2048), ldstr(sl + wa[i] + ks * 2048 + 512));
                if ((ks & 1) == t) stat1(wf, cs[i]);
                acc[i] = SWAP ? MFMA(xf, wf, acc[i]) : MFMA(wf, xf, acc[i]);
            }
        }
    }
}

__global__ __launch_bounds__(512, 2) void k1_qkv(const char* __restrict__ x, const char* __restrict__ wqkv, char* __restrict__ ws_) {
    extern __shared__ __attribute__((aligned(16))) char smem[];
    lptr lds = (lptr)smem;
    const int tid = threadIdx.x, lane = tid & 63, w = rfl(tid >> 6);
    const int b = blockIdx.x, xcd = b & 7, li = b >> 3;
    const int rb = (xcd >> 1) * 8 + (li >> 2), hq = (xcd & 1) * 4 + (li & 3);
    const int start = ((li >> 2) + (li & 3) * 2 + xcd) & 7;
    const int t = w & 1, cg = (w >> 1) & 1, ab = w >> 2;
    const int drow = 8 * w + (lane >> 3), pos = lane & 7;
    const unsigned xoff = drow * 1024 + ((pos ^ ((drow >> 1) & 7)) << 4);
    const unsigned woff = drow * 3072 + ((pos ^ (((drow >> 1) & 1) << 2)) << 4);
    const char* xs = x + (size_t)rb * 64 * 1024;
    const char* wsrc = wqkv + hq * 128;
    const unsigned ldsw = (unsigned)(size_t)lds + w * 1024;
    f32x16 acc[2] = {};
    float s1 = 0.f, s2 = 0.f, cs[2] = {0.f, 0.f};
    const int u = cg * 2 + ab;
    if (ab) k1_loop<1, false>(lds, xs, wsrc, xoff, woff, ldsw, start, lane, t, 1, cg, u, acc, s1, s2, cs);
    else if (cg) k1_loop<2, true>(lds, xs, wsrc, xoff, woff, ldsw, start, lane, t, 2, 0, u, acc, s1, s2, cs);
    else k1_loop<2, false>(lds, xs, wsrc, xoff, woff, ldsw, start, lane, t, 0, 0, u, acc, s1, s2, cs);
    LAS f32x2* sp = (LAS f32x2*)lds;
    LAS float* cp = (LAS float*)(lds + 4096);
    LAS float* mean_s = (LAS float*)(lds + 8192);
    LAS float* rstd_s = mean_s + 64;
    LAS float* csum_s = mean_s + 128;
    sp[w * 64 + lane] = (f32x2){s1, s2};
    cp[(w * 2) * 64 + lane] = cs[0];
    cp[(w * 2 + 1) * 64 + lane] = cs[1];
    LBAR();
    if (tid < 64) {
        const int tt = tid >> 5, r = tid & 31;
        float a1 = 0.f, a2 = 0.f;
#pragma unroll
        for (int i = 0; i < 4; ++i) {
            f32x2 v0 = sp[(tt + 2 * i) * 64 + r], v1 = sp[(tt + 2 * i) * 64 + 32 + r];
            a1 += v0[0] + v1[0]; a2 += v0[1] + v1[1];
        }
        const float m = a1 * (1.f / D);
        const float var = fmaxf(a2 * (1.f / D) - m * m, 0.f);
        mean_s[tid] = m; rstd_s[tid] = __builtin_amdgcn_rsqf(var + 1e-5f);
    } else if (tid < 256) {
        const int c = tid - 64, i = c >> 5, r = c & 31;
        const int wb = i < 2 ? 0 : (i == 2 ? 4 : (i == 3 ? 6 : 2));
        const int un = i < 2 ? i : (i < 4 ? 0 : i - 4);
        csum_s[c] = cp[(wb * 2 + un) * 64 + r] + cp[(wb * 2 + un) * 64 + 32 + r] + cp[((wb + 1) * 2 + un) * 64 + r] + cp[((wb + 1) * 2 + un) * 64 + 32 + r];
    }
    LBAR();
    const int r = lane & 31, h = lane >> 5;
    const int tile = rb * 2 + t;
    if (ab || !cg) {
        const float m = mean_s[32 * t + r];
        const float rs = rstd_s[32 * t + r] * (ab ? 1.f : 0.25f * 1.4426950408889634f);
        char* dst = ws_ + (ab ? WS_K : WS_Q);
        const int nu = ab ? 1 : 2;
#pragma unroll
        for (int i = 0; i < 2; ++i) if (i < nu) {
            const int ci = ab ? 2 + cg : i, hp = ab ? cg : i;
#pragma unroll
            for (int e = 0; e < 2; ++e) {
                f32x4 c0 = *(LAS f32x4*)(csum_s + ci * 32 + 16 * e + 4 * h), c1 = *(LAS f32x4*)(csum_s + ci * 32 + 16 * e + 8 + 4 * h);
                float v[8];
#pragma unroll
                for (int j = 0; j < 4; ++j) { v[j] = rs * (acc[i][8 * e + j] - m * c0[j]); v[4 + j] = rs * (acc[i][8 * e + 4 + j] - m * c1[j]); }
                const int head = hq * 4 + hp * 2 + e;
                *(u32x4*)(dst + ((size_t)(head * 64 + tile) * 64 + lane) * 16) = (u32x4){pk(v[0], v[1]), pk(v[2], v[3]), pk(v[4], v[5]), pk(v[6], v[7])};
            }
        }
    } else {
        f32x4 mm[4], rr[4];
#pragma unroll
        for (int g = 0; g < 4; ++g) { mm[g] = *(LAS f32x4*)(mean_s + 32 * t + 8 * g + 4 * h); rr[g] = *(LAS f32x4*)(rstd_s + 32 * t + 8 * g + 4 * h); }
#pragma unroll
        for (int i = 0; i < 2; ++i) {
            const float c = csum_s[(4 + i) * 32 + r];
            const int head = hq * 4 + i * 2 + (r >> 4);
#pragma unroll
            for (int s = 0; s < 2; ++s) {
                float v[8];
#pragma unroll
                for (int j = 0; j < 4; ++j) {
                    v[j] = rr[2 * s][j] * (acc[i][8 * s + j] - mm[2 * s][j] * c);
                    v[4 + j] = rr[2 * s + 1][j] * (acc[i][8 * s + 4 + j] - mm[2 * s + 1][j] * c);
                }
                *(u32x4*)(ws_ + WS_V + (size_t)(head * 64 + tile) * 1024 + s * 512 + h * 256 + (r & 15) * 16) =
                    (u32x4){pk(v[0], v[1]), pk(v[2], v[3]), pk(v[4], v[5]), pk(v[6], v[7])};
            }
        }
    }
}

constexpr int A_K = 0, A_V = 65536, A_C = 131072, A_X = A_C + 4096, A_FLAG = A_X + 8 * 2304, A_LDS = A_FLAG + 16;

__device__ __forceinline__ void pexp(const f32x16& s, bf16x8& p0, bf16x8& p1) {
    float e[16];
#pragma unroll
    for (int i = 0; i < 16; ++i) e[i] = __builtin_amdgcn_exp2f(s[i]);
    p0 = __builtin_bit_cast(bf16x8, (u32x4){pk(e[0], e[1]), pk(e[2], e[3]), pk(e[4], e[5]), pk(e[6], e[7])});
    p1 = __builtin_bit_cast(bf16x8, (u32x4){pk(e[8], e[9]), pk(e[10], e[11]), pk(e[12], e[13]), pk(e[14], e[15])});
}

__global__ __launch_bounds__(512, 2) void k2_attn(char* __restrict__ ws_, const char* __restrict__ wo, const char* __restrict__ wg,
                                                  const char* __restrict__ wu, const char* __restrict__ wd) {
    extern __shared__ __attribute__((aligned(16))) char smem[];
    lptr lds = (lptr)smem;
    const int tid = threadIdx.x, lane = tid & 63, w = rfl(tid >> 6);
    const int b = blockIdx.x, head = (b & 7) * 4 + ((b >> 3) & 3), qb = b >> 5;
    const int qp = w & 3, kh = w >> 2, r = lane & 31, h = lane >> 5;
    const char* qsrc = ws_ + WS_Q + (size_t)(head * 64 + qb * 8 + qp * 2) * 1024 + lane * 16;
    const bf16x8 q0 = *(const bf16x8*)qsrc, q1 = *(const bf16x8*)(qsrc + 1024);
    {
        const char* ks = ws_ + WS_K + (size_t)(head * 64 + kh * 32 + qp) * 1024;
        const unsigned l = (unsigned)(size_t)lds + (kh * 32 + qp) * 1024;
#pragma unroll
        for (int c = 0; c < 8; ++c) {
            glds(ks + c * 4096, lane * 16, l + A_K + c * 4096);
            glds(ks + (WS_V - WS_K) + c * 4096, lane * 16, l + A_V + c * 4096);
        }
    }
    if (tid < 64) {
        const int pos = tid >> 3, sl = tid & 7;
        const unsigned v = (sl == 0 || sl == 4) ? ONES2 : 0u;
        *(LAS u32x4*)(lds + A_C + pos * 512 + sl * 16) = (u32x4){v, v, v, v};
    }
    if (tid == 64) *(LAS unsigned*)(lds + A_FLAG) = 0u;
    unsigned ka = A_K + kh * 32768 + lane * 16;
    unsigned va, vstep;
    if (r < 16) { va = A_V + kh * 32768 + h * 256 + r * 16; vstep = 4096; }
    else { va = A_C + ((r >= 20 && r < 28) ? (r == 20 ? 64 : 80) : (r == 16 ? 0 : 16)); vstep = 0; }
    const unsigned ka0 = ka, va0 = va;
    const int job = b * 8 + w;
    const char* jsrc; int jstride; char* jdst; int jks;
    if (job < 896) { const int cgp = job >> 5, ks = job & 31; jsrc = wg + (size_t)ks * 16 * 3584 + cgp * 128; jstride = 3584; jdst = ws_ + WS_WGU + (size_t)(cgp * 4 * 32 + ks) * 1024; jks = 32; }
    else if (job < 1792) { const int j = job - 896, lc = j / 112, ks = j - lc * 112; jsrc = wd + (size_t)ks * 16 * 1024 + lc * 128; jstride = 1024; jdst = ws_ + WS_WD + (size_t)(lc * 2 * 112 + ks) * 1024; jks = 112; }
    else { const int j = job - 1792, lc = j >> 5, ks = j & 31; jsrc = wo + (size_t)ks * 16 * 1024 + lc * 128; jstride = 1024; jdst = ws_ + WS_WO + (size_t)(lc * 2 * 32 + ks) * 1024; jks = 32; }
    u32x4 jr[4];

    f32x16 o0 = {}, o1 = {};
    const f32x16 zero = {};
    for (int c = 0; c < 8; ++c) {
        if (c == 0) WAITBAR(14);
        else if (c == 1) {
            WAITBAR(10);
            const size_t lo = (size_t)(lane >> 3) * jstride + (lane & 7) * 16;
            jr[0] = *(const u32x4*)(jsrc + lo); jr[1] = *(const u32x4*)(jsrc + lo + 8 * jstride);
            if (job < 896) { const char* j2 = jsrc + (wu - wg); jr[2] = *(const u32x4*)(j2 + lo); jr[3] = *(const u32x4*)(j2 + lo + 8 * jstride); }
        } else if (c == 3) WAITBAR(0);
        else if (c == 5) {
            lptr st = lds + A_X + w * 2304;
            const int krow = lane >> 3, ch = lane & 7;
            const int q = (lane & 15) >> 2, p = lane & 3, blk = (lane >> 4) & 1;
            if (job < 896) {
#pragma unroll
                for (int rr = 0; rr < 2; ++rr) {
                    if ((ch >> 2) == rr) {
                        const int o = ((ch >> 1) & 1) * 1024 + krow * 64 + (ch & 1) * 16;
                        *(LAS u32x4*)(st + o) = jr[0]; *(LAS u32x4*)(st + o + 512) = jr[1];
                        *(LAS u32x4*)(st + o + 32) = jr[2]; *(LAS u32x4*)(st + o + 512 + 32) = jr[3];
                    }
#pragma unroll
                    for (int i = 0; i < 2; ++i) {
                        lptr a = st + i * 1024 + (4 * h + q) * 64 + blk * 32 + p * 8;
                        bf16x8 f = cat(ldstr(a), ldstr(a + 512));
                        *(bf16x8*)(jdst + (size_t)(2 * rr + i) * 32 * 1024 + lane * 16) = f;
                    }
                }
            } else {
                const int o = (ch >> 2) * 1024 + krow * 64 + (ch & 3) * 16;
                *(LAS u32x4*)(st + o) = jr[0]; *(LAS u32x4*)(st + o + 512) = jr[1];
#pragma unroll
                for (int i = 0; i < 2; ++i) {
                    lptr a = st + i * 1024 + (4 * h + q) * 64 + blk * 32 + p * 8;
                    bf16x8 f = cat(ldstr(a), ldstr(a + 512));
                    *(bf16x8*)(jdst + (size_t)i * jks * 1024 + lane * 16) = f;
                }
            }
        }
#pragma unroll
        for (int i = 0; i < 4; ++i) {
            const bf16x8 kf = lds128(lds + ka + i * 1024);
            const bf16x8 v0 = lds128(lds + va + i * 1024), v1 = lds128(lds + va + i * 1024 + 512);
            f32x16 s0 = MFMA(kf, q0, zero), s1 = MFMA(kf, q1, zero);
            bf16x8 pa, pb;
            pexp(s0, pa, pb);
            o0 = MFMA(v0, pa, o0); o0 = MFMA(v1, pb, o0);
            pexp(s1, pa, pb);
            o1 = MFMA(v0, pa, o1); o1 = MFMA(v1, pb, o1);
        }
        ka += 4096; va += vstep;
    }
    LAS float* xo = (LAS float*)(lds + A_X + w * 2304);
    LAS float* xi = (LAS float*)(lds + A_X + (w ^ 4) * 2304);
    char* odst = ws_ + WS_O + ((size_t)(qb * 8 + qp * 2 + kh) * 32 + head) * 1024 + lane * 16;
    {
#pragma unroll
        for (int i = 0; i < 9; ++i) xo[i * 64 + lane] = kh ? o0[i] : o1[i];
        LBAR();
        float o[9];
        bool ok = true;
#pragma unroll
        for (int i = 0; i < 9; ++i) { o[i] = (kh ? o1[i] : o0[i]) + xi[i * 64 + lane]; ok = ok && (fabsf(o[i]) <= 3.0e38f); }
        ok = ok && (o[8] >= 0x1p-30f) && (o[8] <= 0x1p100f);
        const float inv = __builtin_amdgcn_rcpf(o[8]);
        *(u32x4*)odst = (u32x4){pk(o[0] * inv, o[1] * inv), pk(o[2] * inv, o[3] * inv), pk(o[4] * inv, o[5] * inv), pk(o[6] * inv, o[7] * inv)};
        if (!ok) *(LAS unsigned*)(lds + A_FLAG) = 1u;
        LBAR();
    }
    if (rfl(*(LAS unsigned*)(lds + A_FLAG))) {
        float m0 = -INFINITY, m1 = -INFINITY;
        ka = ka0;
        for (int i = 0; i < 32; ++i) {
            const bf16x8 kf = lds128(lds + ka);
            f32x16 s0 = MFMA(kf, q0, zero), s1 = MFMA(kf, q1, zero);
#pragma unroll
            for (int j = 0; j < 16; ++j) { m0 = fmaxf(m0, s0[j]); m1 = fmaxf(m1, s1[j]); }
            ka += 1024;
        }
        m0 = fmaxf(m0, __shfl_xor(m0, 32)); m1 = fmaxf(m1, __shfl_xor(m1, 32));
        xo[lane] = m0; xo[64 + lane] = m1;
        LBAR();
        m0 = fmaxf(m0, xi[lane]); m1 = fmaxf(m1, xi[64 + lane]);
        LBAR();
        f32x16 n0, n1;
#pragma unroll
        for (int j = 0; j < 16; ++j) { n0[j] = -m0; n1[j] = -m1; }
        o0 = zero; o1 = zero; ka = ka0; va = va0;
        for (int c = 0; c < 8; ++c) {
#pragma unroll 1
            for (int i = 0; i < 4; ++i) {
                const bf16x8 kf = lds128(lds + ka + i * 1024);
                const bf16x8 v0 = lds128(lds + va + i * 1024), v1 = lds128(lds + va + i * 1024 + 512);
                f32x16 s0 = MFMA(kf, q0, n0), s1 = MFMA(kf, q1, n1);
                bf16x8 pa, pb;
                pexp(s0, pa, pb);
                o0 = MFMA(v0, pa, o0); o0 = MFMA(v1, pb, o0);
                pexp(s1, pa, pb);
                o1 = MFMA(v0, pa, o1); o1 = MFMA(v1, pb, o1);
            }
            ka += 4096; va += vstep;
        }
#pragma unroll
        for (int i = 0; i < 9; ++i) xo[i * 64 + lane] = kh ? o0[i] : o1[i];
        LBAR();
        float o[9];
#pragma unroll
        for (int i = 0; i < 9; ++i) o[i] = (kh ? o1[i] : o0[i]) + xi[i * 64 + lane];
        const float inv = __builtin_amdgcn_rcpf(o[8]);
        *(u32x4*)odst = (u32x4){pk(o[0] * inv, o[1] * inv), pk(o[2] * inv, o[3] * inv), pk(o[4] * inv, o[5] * inv), pk(o[6] * inv, o[7] * inv)};
    }
}

template <int NKS, bool FINAL>
__global__ __launch_bounds__(256, 1) void k_gemm64(const char* __restrict__ wt, const char* __restrict__ at, const char* __restrict__ res, char* __restrict__ out) {
    extern __shared__ __attribute__((aligned(16))) char smem[];
    lptr lds = (lptr)smem;
    constexpr int NST = NKS / 8;
    const int tid = threadIdx.x, lane = tid & 63, w = rfl(tid >> 6);
    const int b = blockIdx.x, xcd = b & 7, li = b >> 3;
    const int rb = xcd * 4 + (li >> 3), cb = li & 7;
    const int tt = w & 1, ct = w >> 1, r = lane & 31, h = lane >> 5;
    const int ttile = rb * 2 + tt, ctile = cb * 2 + ct;
    u32x2 rx[4]; u32x4 rh[2];
    if (FINAL) {
        const char* p = res + ((size_t)ttile * 32 + ctile * 2) * 1024 + lane * 16;
        rh[0] = *(const u32x4*)p; rh[1] = *(const u32x4*)(p + 1024);
    } else {
        const char* p = res + (size_t)(ttile * 32 + r) * 1024 + (ctile * 32 + 4 * h) * 2;
#pragma unroll
        for (int g = 0; g < 4; ++g) rx[g] = *(const u32x2*)(p + g * 16);
    }
    const char* src = (w < 2) ? wt + (size_t)(cb * 2 + w) * NKS * 1024 : at + (size_t)(rb * 2 + w - 2) * NKS * 1024;
    const unsigned ldsw = (unsigned)(size_t)lds + w * 8192;
    auto issue = [&](int st) {
        const char* s = src + (size_t)st * 8192;
        const unsigned l = ldsw + (st & 3) * 32768;
#pragma unroll
        for (int i = 0; i < 8; ++i) glds(s + i * 1024, lane * 16, l + i * 1024);
    };
    issue(0); issue(1); issue(2); issue(3);
    f32x16 acc = {};
    const unsigned aa = ct * 8192 + lane * 16, ba = 16384 + tt * 8192 + lane * 16;
#pragma unroll
    for (int st = 0; st < NST; ++st) {
        if (st == 0) WAITBAR(24); else if (st < NST - 2) WAITBAR(16); else if (st == NST - 2) WAITBAR(8); else WAITBAR(0);
        if (st >= 1 && st + 3 < NST) issue(st + 3);
        lptr sl = lds + (st & 3) * 32768;
#pragma unroll
        for (int ks = 0; ks < 8; ++ks) acc = MFMA(lds128(sl + aa + ks * 1024), lds128(sl + ba + ks * 1024), acc);
    }
    if (FINAL) {
        char* p = out + (size_t)(ttile * 32 + r) * 1024 + (ctile * 32 + 4 * h) * 2;
#pragma unroll
        for (int g = 0; g < 4; ++g) {
            const unsigned a = rh[g >> 1][(g & 1) * 2], c = rh[g >> 1][(g & 1) * 2 + 1];
            *(u32x2*)(p + g * 16) = (u32x2){pk(acc[4 * g] + bflo(a), acc[4 * g + 1] + bfhi(a)), pk(acc[4 * g + 2] + bflo(c), acc[4 * g + 3] + bfhi(c))};
        }
    } else {
        char* p = out + ((size_t)ttile * 32 + ctile * 2) * 1024 + lane * 16;
        unsigned o[8];
#pragma unroll
        for (int g = 0; g < 4; ++g) {
            o[2 * g] = pk(acc[4 * g] + bflo(rx[g][0]), acc[4 * g + 1] + bfhi(rx[g][0]));
            o[2 * g + 1] = pk(acc[4 * g + 2] + bflo(rx[g][1]), acc[4 * g + 3] + bfhi(rx[g][1]));
        }
        *(u32x4*)p = (u32x4){o[0], o[1], o[2], o[3]};
        *(u32x4*)(p + 1024) = (u32x4){o[4], o[5], o[6], o[7]};
    }
}

constexpr int K4_SLOT = 45056;
template <int NU>
__device__ __forceinline__ void k4_body(lptr lds, const char* hsrc, const char* wsrc, char* act, int w, int lane, int g, int j) {
    constexpr int NI = NU + 2;
    const int t = w & 3, f0 = (NU == 4) ? 0 : 4;
    const int r = lane & 31, h = lane >> 5;
    const char* src[NI];
#pragma unroll
    for (int i = 0; i < NI; ++i) {
        const int n = w + 8 * i, piece = n >> 2, sub = n & 3;
        src[i] = (piece < 4 ? hsrc + (size_t)piece * 32768 : wsrc + (size_t)(piece - 4) * 32768) + sub * 1024;
    }
    const unsigned ldsw = (unsigned)(size_t)lds + w * 1024;
    auto issue = [&](int st) {
        const unsigned l = ldsw + (st % 3) * K4_SLOT;
#pragma unroll
        for (int i = 0; i < NI; ++i) glds(src[i] + st * 4096, lane * 16, l + i * 8192);
    };
    issue(0); issue(1); issue(2);
    f32x16 acc[NU] = {};
    float s1 = 0.f, s2 = 0.f, cs[NU] = {};
    const unsigned ha = t * 4096 + lane * 16, wa = 16384 + f0 * 4096 + lane * 16;
#pragma unroll
    for (int st = 0; st < 8; ++st) {
        if (NU == 4) { if (st == 0) WAITBAR(12); else if (st < 7) WAITBAR(6); else WAITBAR(0); }
        else { if (st == 0) WAITBAR(10); else if (st < 7) WAITBAR(5); else WAITBAR(0); }
        if (st >= 1 && st + 2 < 8) issue(st + 2);
        lptr sl = lds + (st % 3) * K4_SLOT;
#pragma unroll
        for (int ks = 0; ks < 4; ++ks) {
            const bf16x8 hf = lds128(sl + ha + ks * 1024);
            if ((ks & 1) == (NU == 4 ? 0 : 1)) stat2(hf, s1, s2);
#pragma unroll
            for (int i = 0; i < NU; ++i) {
                const bf16x8 wf = lds128(sl + wa + i * 4096 + ks * 1024);
                if (ks == t) stat1(wf, cs[i]);
                acc[i] = MFMA(wf, hf, acc[i]);
            }
        }
    }
    LAS f32x2* sp = (LAS f32x2*)lds;
    LAS float* cp = (LAS float*)(lds + 4096);
    LAS float* csum = (LAS float*)(lds + 4096 + 7168);
    sp[w * 64 + lane] = (f32x2){s1, s2};
#pragma unroll
    for (int i = 0; i < NU; ++i) cp[((f0 + i) * 4 + t) * 64 + lane] = cs[i];
    LBAR();
    const int tid = w * 64 + lane;
    if (tid < 224) {
        const int f = tid >> 5;
        float a = 0.f;
#pragma unroll
        for (int i = 0; i < 4; ++i) a += cp[(f * 4 + i) * 64 + r] + cp[(f * 4 + i) * 64 + 32 + r];
        csum[tid] = a;
    }
    const f32x2 p0 = sp[t * 64 + r], p1 = sp[t * 64 + 32 + r], p2 = sp[(t + 4) * 64 + r], p3 = sp[(t + 4) * 64 + 32 + r];
    const float m = (p0[0] + p1[0] + p2[0] + p3[0]) * (1.f / D);
    const float var = fmaxf((p0[1] + p1[1] + p2[1] + p3[1]) * (1.f / D) - m * m, 0.f);
    const float rs = __builtin_amdgcn_rsqf(var + 1e-5f);
    LBAR();
    constexpr float C1 = -2.f * 0.7978845608028654f * 1.4426950408889634f, C2 = C1 * 0.044715f;
#pragma unroll
    for (int i = 0; i < NU; ++i) {
        LAS float* c = csum + (f0 + i) * 32 + 4 * h;
        const f32x4 g0 = *(LAS f32x4*)c, g1 = *(LAS f32x4*)(c + 8), u0 = *(LAS f32x4*)(c + 16), u1 = *(LAS f32x4*)(c + 24);
        float a[8];
#pragma unroll
        for (int e = 0; e < 8; ++e) {
            const float gv = rs * (acc[i][e] - m * (e < 4 ? g0[e & 3] : g1[e & 3]));
            const float uv = rs * (acc[i][8 + e] - m * (e < 4 ? u0[e & 3] : u1[e & 3]));
            const float ex = __builtin_amdgcn_exp2f(gv * (C1 + C2 * gv * gv));
            a[e] = gv * uv * __builtin_amdgcn_rcpf(1.f + ex);
        }
        *(u32x4*)(act + ((size_t)(g * 4 + t) * 112 + j * 7 + f0 + i) * 1024 + lane * 16) = (u32x4){pk(a[0], a[1]), pk(a[2], a[3]), pk(a[4], a[5]), pk(a[6], a[7])};
    }
}

__global__ __launch_bounds__(512, 2) void k4_upgate(char* __restrict__ ws_) {
    extern __shared__ __attribute__((aligned(16))) char smem[];
    lptr lds = (lptr)smem;
    const int tid = threadIdx.x, lane = tid & 63, w = rfl(tid >> 6);
    const int b = blockIdx.x, xcd = b & 7, li = b >> 3;
    const int g = xcd * 2 + (li >> 4), j = li & 15;
    const char* hsrc = ws_ + WS_H + (size_t)g * 4 * 32768;
    const char* wsrc = ws_ + WS_WGU + (size_t)j * 7 * 32768;
    if (w < 4) k4_body<4>(lds, hsrc, wsrc, ws_ + WS_ACT, w, lane, g, j);
    else k4_body<3>(lds, hsrc, wsrc, ws_ + WS_ACT, w, lane, g, j);
}

extern "C" void kernel_launch(void* const* d_in, const int* in_sizes, int n_in, void* d_out, int out_size, void* d_ws, size_t ws_size,
                              hipStream_t stream) {
    const char* x = (const char*)d_in[0];
    const char* wqkv = (const char*)d_in[1];
    const char* wo = (const char*)d_in[2];
    const char* wg = (const char*)d_in[4];
    const char* wu = (const char*)d_in[5];
    const char* wd = (const char*)d_in[6];
    char* ws = (char*)d_ws;
    (void)hipFuncSetAttribute((const void*)k1_qkv, hipFuncAttributeMaxDynamicSharedMemorySize, 4 * K1_SLOT);
    (void)hipFuncSetAttribute((const void*)k2_attn, hipFuncAttributeMaxDynamicSharedMemorySize, A_LDS);
    (void)hipFuncSetAttribute((const void*)k_gemm64<32, false>, hipFuncAttributeMaxDynamicSharedMemorySize, 131072);
    (void)hipFuncSetAttribute((const void*)k4_upgate, hipFuncAttributeMaxDynamicSharedMemorySize, 3 * K4_SLOT);
    (void)hipFuncSetAttribute((const void*)k_gemm64<112, true>, hipFuncAttributeMaxDynamicSharedMemorySize, 131072);
    k1_qkv<<<256, 512, 4 * K1_SLOT, stream>>>(x, wqkv, ws);
    k2_attn<<<256, 512, A_LDS, stream>>>(ws, wo, wg, wu, wd);
    k_gemm64<32, false><<<256, 256, 131072, stream>>>(ws + WS_WO, ws + WS_O, x, ws + WS_H);
    k4_upgate<<<256, 512, 3 * K4_SLOT, stream>>>(ws);
    k_gemm64<112, true><<<256, 256, 131072, stream>>>(ws + WS_WD, ws + WS_ACT, ws + WS_H, (char*)d_out);
}
```

```cpp
#include <hip/hip_runtime.h>
#include <hip/hip_bf16.h>
#include <stdint.h>

typedef __attribute__((ext_vector_type(8))) short bf16x8;
typedef __attribute__((ext_vector_type(4))) short s16x4;
typedef __attribute__((ext_vector_type(16))) float f32x16;
typedef __attribute__((ext_vector_type(4))) float f32x4;
typedef __attribute__((ext_vector_type(2))) float f32x2;
typedef __attribute__((ext_vector_type(4))) unsigned u32x4;
typedef __attribute__((ext_vector_type(2))) unsigned u32x2;
typedef __attribute__((ext_vector_type(2))) __bf16 bf16x2;
typedef unsigned long long u64;
#define LAS __attribute__((address_space(3)))
#define GAS __attribute__((address_space(1)))
typedef LAS char* lptr;

#define MFMA(a, b, c) __builtin_amdgcn_mfma_f32_32x32x16_bf16(a, b, c, 0, 0, 0)
#define SBAR() __builtin_amdgcn_sched_barrier(0)
#define WAITBAR(N) asm volatile("s_waitcnt vmcnt(%0) lgkmcnt(0)\n\ts_barrier" :: "i"(N) : "memory")
#define LBAR() asm volatile("s_waitcnt lgkmcnt(0)\n\ts_barrier" ::: "memory")
#define VMWAIT(N) asm volatile("s_waitcnt vmcnt(%0)" :: "i"(N) : "memory")
#define VMCLR() __builtin_amdgcn_s_waitcnt(0x0F70)
#define RLX __ATOMIC_RELAXED, __HIP_MEMORY_SCOPE_AGENT

#define STAMP_DECL do {} while (0)
#define RCARG
#define RC(i)
#define RCADD(n) do {} while (0)
#define RCINC(i, n) do {} while (0)
#define STAMP(idx) do {} while (0)
#define STAMPM(m, idx) do {} while (0)
#define STAMP_FLUSH(ws) do {} while (0)

constexpr int D = 512;
constexpr unsigned WS_Q = 0;
constexpr unsigned WS_K = WS_Q + (2u << 20);
constexpr unsigned WS_V = WS_K + (2u << 20);
constexpr unsigned WS_O = WS_V + (2u << 20);
constexpr unsigned WS_H = WS_O + (2u << 20);
constexpr unsigned WS_ACT = WS_H + (2u << 20);
constexpr unsigned WS_WO = WS_ACT + (7u << 20);
constexpr unsigned WS_WGU = WS_WO + (512u << 10);
constexpr unsigned WS_WD = WS_WGU + (3584u << 10);
constexpr unsigned WS_HS = WS_WD + (1792u << 10);
constexpr unsigned WS_CS = WS_HS + (256u << 10);
constexpr unsigned WS_FL = WS_CS + (256u << 10);
constexpr unsigned WS_END = WS_FL + 65536;
constexpr int FL_P = 0, FL_1 = 1024, FL_R = 2048, FL_2 = 3072, FL_3 = 5120, FL_4 = 6144, FL_E = 7168;
constexpr unsigned POISON = 0xFFFFFFFFu;
constexpr int S0 = 0, S1 = 32768, S2 = 65536, S3 = 98304, M0 = 131072, LDS_BYTES = 163840;
constexpr int M_Q = M0, M_X = M0, M_STG = M0 + 18432, M_CONST = M0 + 26624, M_WORD = M0 + 30720, M_CNT = M0 + 16384;
constexpr int P1_W = 65536, P1_SLOT = 24576;

__device__ __forceinline__ void glds3(const void* sbase, unsigned voff, unsigned ldsaddr) {
    asm volatile("s_mov_b32 m0, %2\n\ts_nop 0\n\tglobal_load_lds_dwordx4 %0, %1\n\tglobal_load_lds_dwordx4 %0, %1 offset:1024\n\tglobal_load_lds_dwordx4 %0, %1 offset:2048"
                 :: "v"(voff), "s"(sbase), "s"(ldsaddr) : "memory");
}
__device__ __forceinline__ void glds3_upper(int w, const void* sbase, unsigned voff, unsigned ldsaddr) {
    asm volatile("s_cmp_lt_u32 %3, 4\n\ts_cbranch_scc1 1f\n\ts_mov_b32 m0, %2\n\ts_nop 0\n\tglobal_load_lds_dwordx4 %0, %1\n\t"
                 "global_load_lds_dwordx4 %0, %1 offset:1024\n\tglobal_load_lds_dwordx4 %0, %1 offset:2048\n1:"
                 :: "v"(voff), "s"(sbase), "s"(ldsaddr), "s"(w) : "scc", "memory");
}
__device__ __forceinline__ void gld2(const void* sbase, unsigned voff, unsigned ldsaddr) {
    asm volatile("s_mov_b32 m0, %2\n\ts_nop 0\n\tglobal_load_lds_dwordx4 %0, %1 sc1\n\tglobal_load_lds_dwordx4 %0, %1 offset:1024 sc1"
                 :: "v"(voff), "s"(sbase), "s"(ldsaddr) : "memory");
}
__device__ __forceinline__ void gld4(const void* sbase, unsigned voff, unsigned ldsaddr) {
    asm volatile("s_mov_b32 m0, %2\n\ts_nop 0\n\tglobal_load_lds_dwordx4 %0, %1 sc1\n\tglobal_load_lds_dwordx4 %0, %1 offset:1024 sc1\n\t"
                 "global_load_lds_dwordx4 %0, %1 offset:2048 sc1\n\tglobal_load_lds_dwordx4 %0, %1 offset:3072 sc1"
                 :: "v"(voff), "s"(sbase), "s"(ldsaddr) : "memory");
}
__device__ __forceinline__ void gld8(const char* sbase, unsigned voff, unsigned ldsaddr) {
    asm volatile("s_mov_b32 m0, %2\n\ts_nop 0\n\tglobal_load_lds_dwordx4 %0, %1 offset:-4096 sc1\n\tglobal_load_lds_dwordx4 %0, %1 offset:-3072 sc1\n\t"
                 "global_load_lds_dwordx4 %0, %1 offset:-2048 sc1\n\tglobal_load_lds_dwordx4 %0, %1 offset:-1024 sc1\n\t"
                 "global_load_lds_dwordx4 %0, %1 sc1\n\tglobal_load_lds_dwordx4 %0, %1 offset:1024 sc1\n\t"
                 "global_load_lds_dwordx4 %0, %1 offset:2048 sc1\n\tglobal_load_lds_dwordx4 %0, %1 offset:3072 sc1"
                 :: "v"(voff), "s"(sbase + 4096), "s"(ldsaddr + 4096) : "memory");
}
__device__ __forceinline__ bf16x8 lds128(lptr p) { return *(const LAS bf16x8*)p; }
__device__ __forceinline__ s16x4 ldstr(lptr p) { return __builtin_amdgcn_ds_read_tr16_b64_v4i16((LAS s16x4*)p); }
__device__ __forceinline__ bf16x8 cat(s16x4 a, s16x4 b) { return __builtin_shufflevector(a, b, 0, 1, 2, 3, 4, 5, 6, 7); }
__device__ __forceinline__ unsigned pk(float lo, float hi) { bf16x2 r = {(__bf16)lo, (__bf16)hi}; return __builtin_bit_cast(unsigned, r); }
__device__ __forceinline__ float dot2(unsigned a, unsigned b, float c) {
    return __builtin_amdgcn_fdot2_f32_bf16(__builtin_bit_cast(bf16x2, a), __builtin_bit_cast(bf16x2, b), c, false);
}
__device__ __forceinline__ float bflo(unsigned u) { return __uint_as_float(u << 16); }
__device__ __forceinline__ float bfhi(unsigned u) { return __uint_as_float(u & 0xffff0000u); }
constexpr unsigned ONES2 = 0x3F803F80u;
__device__ __forceinline__ void stat2(u32x4 u, float& s1, float& s2) {
#pragma unroll
    for (int i = 0; i < 4; ++i) { s1 = dot2(u[i], ONES2, s1); s2 = dot2(u[i], u[i], s2); }
}
__device__ __forceinline__ void stat1(bf16x8 f, float& s1) {
    u32x4 u = __builtin_bit_cast(u32x4, f);
#pragma unroll
    for (int i = 0; i < 4; ++i) s1 = dot2(u[i], ONES2, s1);
}
__device__ __forceinline__ unsigned opaque(unsigned x) { asm volatile("" : "+v"(x)); return x; }
template <int I> struct IC { static constexpr int v = I; };
template <int B, int E, class Fn> __device__ __forceinline__ void sfor(Fn&& f) { if constexpr (B < E) { f(IC<B>{}); sfor<B + 1, E>(f); } }
#define PIN(x) asm volatile("" : "+v"(x))
__device__ __forceinline__ int rfl(int x) { return __builtin_amdgcn_readfirstlane(x); }
__device__ __forceinline__ float halfsum(float x) {
    auto r = __builtin_amdgcn_permlane32_swap(__float_as_uint(x), __float_as_uint(x), false, false);
    return __uint_as_float(r[0]) + __uint_as_float(r[1]);
}
typedef __amdgpu_buffer_rsrc_t rsrc_t;
__device__ __forceinline__ void st16(rsrc_t r, unsigned voff, unsigned soff, u32x4 v) {
    __builtin_amdgcn_raw_buffer_store_b128(v, r, voff, soff, 16);
    asm volatile("s_nop 1" :: "v"(v));
}
__device__ __forceinline__ u32x4 ld16(rsrc_t r, unsigned voff, unsigned soff) { return __builtin_amdgcn_raw_buffer_load_b128(r, voff, soff, 16); }
__device__ __forceinline__ void publish(GAS u64* f, u64 tag, int lane) { if (lane == 0) __hip_atomic_store(f, tag, RLX); }
__device__ __forceinline__ void poll(GAS u64* f, u64 tag) {
#pragma nounroll
    for (unsigned spins = 0; spins < (1u << 16); ++spins) {
        const u64 v = __hip_atomic_load(f, RLX);
        if (__all(v == tag)) break;
        __builtin_amdgcn_s_sleep(2);
    }
    asm volatile("" ::: "memory");
}
__device__ __forceinline__ void poll(GAS u64* f, int n, u64 tag, int lane) { poll(f + (lane & (n - 1)), tag); }
#define SL1 20
#define SL2 8
#define SL3 8
__device__ __forceinline__ unsigned mx(u32x4 v, unsigned m) { return max(max(v[2], v[3]), max(max(v[0], v[1]), m)); }
__device__ __forceinline__ void verify4(u32x4& a, u32x4& b, u32x4& c, u32x4& d, rsrc_t ws, unsigned voff, unsigned oa, unsigned ob, unsigned oc,
                                        unsigned od, GAS u64* flags, int n, u64 tag RCARG) {
    if (__builtin_expect(__any(mx(d, mx(c, mx(b, mx(a, 0u)))) == POISON), 0)) {
#pragma nounroll
        for (int t = 0;; ++t) {
            if (t == 32) poll(flags, n, tag, (int)(voff >> 4)); else __builtin_amdgcn_s_sleep(2);
            a = ld16(ws, voff, oa); b = ld16(ws, voff, ob); c = ld16(ws, voff, oc); d = ld16(ws, voff, od);
            VMCLR();
            RCADD(1);
            if (t == 32 || !__any(mx(d, mx(c, mx(b, mx(a, 0u)))) == POISON)) break;
        }
    }
}
__device__ __forceinline__ void poll128(GAS u64* f, u64 tag, int lane) { poll(f + lane, tag); poll(f + 64 + lane, tag); }
__device__ __forceinline__ bool tags2(u32x4 v, u64 tag) { return v[0] == (unsigned)tag && v[2] == (unsigned)tag && v[1] == (unsigned)(tag >> 32) && v[3] == (unsigned)(tag >> 32); }
#define BC8(x) __builtin_bit_cast(bf16x8, x)
#define BAR() asm volatile("s_barrier" ::: "memory")

__device__ __forceinline__ float sum8(float x) {
    x += __builtin_bit_cast(float, __builtin_amdgcn_mov_dpp(__builtin_bit_cast(int, x), 0xB1, 0xF, 0xF, true));
    x += __builtin_bit_cast(float, __builtin_amdgcn_mov_dpp(__builtin_bit_cast(int, x), 0x4E, 0xF, 0xF, true));
    x += __builtin_bit_cast(float, __builtin_amdgcn_mov_dpp(__builtin_bit_cast(int, x), 0x141, 0xF, 0xF, true));
    return x;
}
#define XWAIT(A, B, C, D, NL, NC) asm volatile("s_cmp_lt_u32 %4, 4\n\ts_cbranch_scc1 1f\n\ts_waitcnt vmcnt(" #NL ")\n\ts_branch 2f\n1:\n\ts_waitcnt vmcnt(" #NC ")\n2:" \
                                               : "+v"(A), "+v"(B), "+v"(C), "+v"(D) : "s"(w) : "scc", "memory")
template <class Side>
__device__ __forceinline__ void p1_norm(lptr lds, const char* xs, int lane, int w, int rot, Side side) {
    const int pos = lane & 7, row = 8 * w + (lane >> 3);
    const unsigned xo = row * 1024 + pos * 16;
    u32x4 xr[8];
#pragma unroll
    for (int c = 0; c < 8; ++c) asm volatile("global_load_dwordx4 %0, %1, %2" : "=v"(xr[c]) : "v"(xo), "s"(xs + ((c + rot) & 7) * 128) : "memory");
    side(-1);
    float s1 = 0.f, s2 = 0.f, t1 = 0.f, t2 = 0.f;
    XWAIT(xr[0], xr[1], xr[2], xr[3], 10, 4);
    stat2(xr[0], s1, s2); stat2(xr[1], t1, t2); stat2(xr[2], s1, s2); stat2(xr[3], t1, t2);
    PIN(s1); PIN(s2); PIN(t1); PIN(t2);
    XWAIT(xr[4], xr[5], xr[6], xr[7], 6, 0);
    stat2(xr[4], s1, s2); stat2(xr[5], t1, t2); stat2(xr[6], s1, s2); stat2(xr[7], t1, t2);
    s1 = sum8(s1 + t1); s2 = sum8(s2 + t2);
    const float m = s1 * (1.f / D);
    const float rs = __builtin_amdgcn_rsqf(fmaxf(s2 * (1.f / D) - m * m, 0.f) + 1e-5f), nb = -m * rs;
    const unsigned a = row * 128 + ((pos ^ ((row >> 1) & 7)) << 4);
#pragma unroll
    for (int c = 0; c < 8; ++c) {
        u32x4 y;
#pragma unroll
        for (int i = 0; i < 4; ++i) y[i] = pk(fmaf(bflo(xr[c][i]), rs, nb), fmaf(bfhi(xr[c][i]), rs, nb));
        *(LAS u32x4*)(lds + a + ((c + rot) & 7) * 8192) = y;
        side(c);
    }
}
__device__ __forceinline__ void p1_store(rsrc_t ws, int lane, int u, int e, int tile, int hq, u32x4 v) {
    const int head0 = hq * 4 + (u & 1) * 2;
    if (u < 4) st16(ws, lane * 16, (u < 2 ? WS_Q : WS_K) + ((head0 + e) * 64 + tile) * 1024, v);
    else st16(ws, ((lane >> 4) & 1) * 65536 + (lane >> 5) * 256 + (lane & 15) * 16, WS_V + (head0 * 64 + tile) * 1024 + e * 512, v);
}
template <int CG>
__device__ __forceinline__ void p1_consume(lptr lds, rsrc_t ws, int lane, int t, int start, int rb, int hq) {
    const int r = lane & 31, h = lane >> 5;
    const int row = 32 * t + r;
    const unsigned xa = row * 128 + ((h ^ ((row >> 1) & 7)) << 4);
    const int q = (lane & 15) >> 2, p = lane & 3, blk = (lane >> 4) & 1;
    unsigned wa[2];
#pragma unroll
    for (int i = 0; i < 2; ++i) wa[i] = P1_W + h * 3072 + q * 128 + (((i * 4 + blk * 2 + (p >> 1)) ^ (((q >> 1) & 1) << 2)) << 4) + (p & 1) * 8;
    f32x16 acc[3] = {};
    bf16x8 xf[4], wf[4][3];
    auto rdx = [&](int slot, unsigned a) { xf[slot] = lds128(lds + a); };
    auto rdw = [&](int slot, int i, lptr p0, lptr p1, int ks) {
        const int u = CG * 3 + i;
        lptr a = ((u & 1) ? p1 : p0) + (u >> 1) * 1024 + ks * 6144;
        wf[slot][i] = cat(ldstr(a), ldstr(a + 512));
    };
    unsigned xc = (start & 7) * 8192;
    lptr c0 = lds + opaque(wa[0]), c1 = lds + opaque(wa[1]);
    LBAR();
#pragma unroll
    for (int ks = 0; ks < 2; ++ks) { rdx(ks, opaque(xc + (xa ^ (ks << 5)))); rdw(ks, 0, c0, c1, ks); rdw(ks, 1, c0, c1, ks); rdw(ks, 2, c0, c1, ks); }
    SBAR();
#pragma unroll 1
    for (int ch = 0; ch < 8; ++ch) {
        const unsigned xn = ((ch + 1 + start) & 7) * 8192, sn = ((ch + 1) & 3) * P1_SLOT;
        lptr n0 = lds + opaque(sn + wa[0]), n1 = lds + opaque(sn + wa[1]);
        const unsigned xa2 = opaque(xc + (xa ^ 64)), xa3 = opaque(xc + (xa ^ 96)), xb0 = opaque(xn + xa), xb1 = opaque(xn + (xa ^ 32));
#pragma unroll
        for (int ks = 0; ks < 4; ++ks) {
            const int s = (ks + 2) & 3;
            if (ks == 2 && ch < 7) LBAR();
#pragma unroll
            for (int i = 0; i < 3; ++i) {
                if (i == 0) rdx(s, ks == 0 ? xa2 : (ks == 1 ? xa3 : (ks == 2 ? xb0 : xb1)));
                if (ks < 2) rdw(s, i, c0, c1, ks + 2); else rdw(s, i, n0, n1, ks - 2);
                SBAR();
                acc[i] = (CG * 3 + i >= 4) ? MFMA(xf[ks], wf[ks][i], acc[i]) : MFMA(wf[ks][i], xf[ks], acc[i]);
                SBAR();
            }
        }
        xc = xn; c0 = n0; c1 = n1;
    }
#pragma unroll
    for (int i = 0; i < 3; ++i) {
        const float sc = (CG * 3 + i < 2) ? 0.25f * 1.4426950408889634f : 1.f;
#pragma unroll
        for (int e = 0; e < 2; ++e) {
            u32x4 v;
#pragma unroll
            for (int j = 0; j < 4; ++j) v[j] = pk(acc[i][8 * e + 2 * j] * sc, acc[i][8 * e + 2 * j + 1] * sc);
            p1_store(ws, lane, CG * 3 + i, e, rb * 2 + t, hq, v);
        }
    }
}

#define A_EXP(d, s) asm volatile("v_exp_f32 %0, %1" : "=v"(d) : "v"(s))
#define A_CVT(d, a, b) asm volatile("v_cvt_pk_bf16_f32 %0, %1, %2" : "=v"(d) : "v"(a), "v"(b))
#define A_QK(d, k, q) asm volatile("v_mfma_f32_32x32x16_bf16 %0, %1, %2, 0" : "=&v"(d) : "v"(k), "v"(q))
#define A_PV(o, v, p) asm volatile("v_mfma_f32_32x32x16_bf16 %0, %1, %2, %0" : "+v"(o) : "v"(v), "v"(p))
#define UNIT(SC, SN, KN, QN, OPREV, VPREV1, OC, VC0, LOADS) do { \
        float e0, e1, e2, e3, e4, e5, e6, e7; u32x4 p0; \
        A_QK(SN, KN, QN); \
        A_EXP(e0, SC[0]); A_EXP(e1, SC[1]); \
        A_PV(OPREV, VPREV1, p1); \
        LOADS; SBAR(); \
        A_EXP(e2, SC[2]); A_EXP(e3, SC[3]); A_CVT(p0[0], e0, e1); \
        A_EXP(e4, SC[4]); A_EXP(e5, SC[5]); A_CVT(p0[1], e2, e3); \
        A_EXP(e6, SC[6]); A_EXP(e7, SC[7]); A_CVT(p0[2], e4, e5); \
        A_EXP(e0, SC[8]); A_CVT(p0[3], e6, e7); A_EXP(e1, SC[9]); \
        A_EXP(e2, SC[10]); A_PV(OC, VC0, p0); A_EXP(e3, SC[11]); A_CVT(p1[0], e0, e1); \
        A_EXP(e4, SC[12]); A_EXP(e5, SC[13]); A_CVT(p1[1], e2, e3); \
        A_EXP(e6, SC[14]); A_EXP(e7, SC[15]); A_CVT(p1[2], e4, e5); \
        asm volatile("s_nop 0"); A_CVT(p1[3], e6, e7); \
    } while (0)

template <class Hook, class Gap>
__device__ __forceinline__ void attn_main(lptr lds, unsigned ka, unsigned va, unsigned vstep, bf16x8 q0, bf16x8 q1, f32x16& o0, f32x16& o1, Hook hook, Gap gap) {
    f32x16 sa, sb;
    u32x4 p1 = {0u, 0u, 0u, 0u};
    bf16x8 kc, kn, va0, va1, vb0, vb1;
    kc = lds128(lds + ka); va0 = lds128(lds + va); va1 = lds128(lds + va + 512);
    vb1 = va1;
    A_QK(sa, kc, q0);
    asm volatile("s_nop 7\n\ts_nop 7" : "+v"(sa), "+v"(p1), "+v"(o0), "+v"(o1));
    auto settle_a = [&]() { asm volatile("s_nop 3" : "+v"(o0), "+v"(o1), "+v"(sa)); };
    auto settle_b = [&]() { asm volatile("s_nop 3" : "+v"(o0), "+v"(o1), "+v"(sb)); };
#pragma unroll
    for (int c = 0; c < 8; ++c) {
        __builtin_amdgcn_s_setprio(3);
        gap(c, 0, settle_a);
        UNIT(sa, sb, kc, q1, o1, vb1, o0, va0, (kn = lds128(lds + ka + 1024), hook(c, 0)));
        gap(c, 1, settle_b);
        UNIT(sb, sa, kn, q0, o0, va1, o1, va0, (vb0 = lds128(lds + va + 1024), vb1 = lds128(lds + va + 1536), hook(c, 1)));
        __builtin_amdgcn_s_setprio(2);
        gap(c, 2, settle_a);
        UNIT(sa, sb, kn, q1, o1, va1, o0, vb0, (kc = lds128(lds + ka + 2048), hook(c, 2)));
        gap(c, 3, settle_b);
        UNIT(sb, sa, kc, q0, o0, vb1, o1, vb0, (va0 = lds128(lds + va + 2048), va1 = lds128(lds + va + 2560), hook(c, 3)));
        __builtin_amdgcn_s_setprio(1);
        gap(c, 4, settle_a);
        UNIT(sa, sb, kc, q1, o1, vb1, o0, va0, (kn = lds128(lds + ka + 3072), hook(c, 4)));
        gap(c, 5, settle_b);
        UNIT(sb, sa, kn, q0, o0, va1, o1, va0, (vb0 = lds128(lds + va + 3072), vb1 = lds128(lds + va + 3584), hook(c, 5)));
        __builtin_amdgcn_s_setprio(0);
        if (c < 7) { ka += 4096; va += vstep; }
        gap(c, 6, settle_a);
        UNIT(sa, sb, kn, q1, o1, va1, o0, vb0, (kc = lds128(lds + ka), hook(c, 6)));
        gap(c, 7, settle_b);
        UNIT(sb, sa, kc, q0, o0, vb1, o1, vb0, (va0 = lds128(lds + va), va1 = lds128(lds + va + 512), hook(c, 7)));
    }
    asm volatile("s_nop 1\n\tv_mfma_f32_32x32x16_bf16 %0, %2, %3, %0\n\ts_nop 7\n\ts_nop 7" : "+v"(o1), "+v"(sa) : "v"(vb1), "v"(p1));
    asm volatile("" : "+v"(o0));
}

__device__ __forceinline__ void pexp(const f32x16& s, float m, bf16x8& p0, bf16x8& p1) {
    float e[16];
#pragma unroll
    for (int i = 0; i < 16; ++i) e[i] = __builtin_amdgcn_exp2f(s[i] - m);
    p0 = __builtin_bit_cast(bf16x8, (u32x4){pk(e[0], e[1]), pk(e[2], e[3]), pk(e[4], e[5]), pk(e[6], e[7])});
    p1 = __builtin_bit_cast(bf16x8, (u32x4){pk(e[8], e[9]), pk(e[10], e[11]), pk(e[12], e[13]), pk(e[14], e[15])});
}

__global__ __launch_bounds__(512, 2) void fused(const char* __restrict__ x, const char* __restrict__ wqkv, const char* __restrict__ wo,
                                                const char* __restrict__ wg, const char* __restrict__ wu, const char* __restrict__ wd,
                                                char* __restrict__ out, char* ws_) {
    extern __shared__ __attribute__((aligned(16))) char smem[];
    lptr lds = (lptr)smem;
    const unsigned lb = (unsigned)(size_t)lds;
    STAMP_DECL; STAMP(0);
    const int tid = threadIdx.x, lane = tid & 63, w = rfl(tid >> 6);
    const int b = blockIdx.x, xcd = b & 7, li = b >> 3;
    const int r = lane & 31, h = lane >> 5;
    const unsigned l16 = lane * 16;
    GAS u64* fl = (GAS u64*)(ws_ + WS_FL);
    const rsrc_t ws = __builtin_amdgcn_make_buffer_rsrc(ws_, 0, WS_END, 0x00020000);
    const int rb1 = (xcd >> 1) * 8 + (li >> 2), hq = (xcd & 1) * 4 + (li & 3);
    const int head = xcd * 4 + (li & 3), qb = li >> 2;
    const int rb = xcd * 4 + (li >> 3), cb = li & 7;
    const int g4 = (xcd >> 2) * 8 + (li >> 2), j4 = (xcd & 3) * 4 + (li & 3);
    const int start1 = ((li >> 2) + (li & 3) * 2 + xcd) & 7;
    const unsigned woff = (8 * (w & 3) + (lane >> 3)) * 3072 + (((lane & 7) ^ ((lane >> 2) & 4)) << 4);
    const char* wsrc = wqkv + hq * 128;
    auto issue = [&](int s) {
        glds3(wsrc + (size_t)((s + 2 * start1) & 15) * 32 * 3072, woff, lb + P1_W + (s & 7) * 12288 + (w & 3) * 3072);
    };
    auto issue_l = [&](int s) {
        glds3_upper(w, wsrc + (size_t)((s + 2 * start1) & 15) * 32 * 3072, woff, lb + P1_W + (s & 7) * 12288 + (w & 3) * 3072);
    };
    p1_norm(lds, x + (size_t)rb1 * 65536, lane, w, (li & 3) * 2, [&](int c) {
        if (c < 0) { issue_l(0); issue_l(1); } else if (c == 2) issue_l(2); else if (c == 6) issue_l(3);
    });
    STAMP(7); STAMPM(1, 1);
    u64 e_raw = __hip_atomic_load(fl + FL_E, RLX);
    asm volatile("" :: "s"(wo), "s"(wg), "s"(wu), "s"(wd), "s"(out));
    const int qp = w & 3, kh = w >> 2;
    const unsigned ka0 = S0 + kh * 32768 + l16;
    unsigned va0, vstep;
    if (r < 16) { va0 = S2 + kh * 32768 + h * 256 + r * 16; vstep = 4096; }
    else { va0 = M_CONST + ((r >= 20 && r < 28) ? (r == 20 ? 64 : 80) : (r == 16 ? 0 : 16)); vstep = 0; }

    const u32x4 poison = {POISON, POISON, POISON, POISON};
    const char* ksrc = ws_ + WS_K + (size_t)head * 65536;

    GAS u64* myf2 = fl + FL_2 + (qb * 8 + qp * 2 + kh) * 32 + head;
    auto attn_finish = [&](f32x16& o0, f32x16& o1, bf16x8 q0, bf16x8 q1, u64 tag) __attribute__((always_inline)) {
    STAMP(5);
    {
        LAS float* xo = (LAS float*)(lds + M_X + w * 2304);
        LAS float* xi = (LAS float*)(lds + M_X + (w ^ 4) * 2304);
        const unsigned odst = WS_O + ((qb * 8 + qp * 2 + kh) * 32 + head) * 1024;
        float mag = 0.f;
#pragma unroll
        for (int i = 0; i < 8; ++i) mag = fmaf(fabsf(o0[i]), 0.0625f, fmaf(fabsf(o1[i]), 0.0625f, mag));
        const bool ok = (mag <= 5.0e36f) & (o0[8] >= 0x1p-31f) & (o0[8] <= 0x1p99f) & (o1[8] >= 0x1p-31f) & (o1[8] <= 0x1p99f);
        if (!ok) *(LAS unsigned*)(lds + M_WORD) = 1u;
#pragma unroll
        for (int i = 0; i < 9; ++i) xo[i * 64 + lane] = kh ? o0[i] : o1[i];
        VMCLR();
        STAMPM(2, 1);
        LBAR();
        STAMPM(2, 2);
        float o[9];
#pragma unroll
        for (int i = 0; i < 9; ++i) o[i] = (kh ? o1[i] : o0[i]) + xi[i * 64 + lane];
        if (rfl(*(LAS unsigned*)(lds + M_WORD))) {
            LBAR();
            const f32x16 zero = {};
            float m0 = -INFINITY, m1 = -INFINITY;
            unsigned ka = ka0, va = va0;
#pragma unroll 1
            for (int i = 0; i < 32; ++i) {
                const bf16x8 kf = lds128(lds + ka);
                const f32x16 s0 = MFMA(kf, q0, zero), s1 = MFMA(kf, q1, zero);
#pragma unroll
                for (int j = 0; j < 16; ++j) { m0 = fmaxf(m0, s0[j]); m1 = fmaxf(m1, s1[j]); }
                ka += 1024;
            }
            m0 = fmaxf(m0, __shfl_xor(m0, 32)); m1 = fmaxf(m1, __shfl_xor(m1, 32));
            xo[lane] = m0; xo[64 + lane] = m1;
            LBAR();
            m0 = fmaxf(m0, xi[lane]); m1 = fmaxf(m1, xi[64 + lane]);
            LBAR();
            o0 = zero; o1 = zero; ka = ka0;
#pragma unroll 1
            for (int i = 0; i < 32; ++i) {
                const unsigned vo = va + (i & 3) * 1024;
                const bf16x8 kf = lds128(lds + ka), v0 = lds128(lds + vo), v1 = lds128(lds + vo + 512);
                bf16x8 pa, pb;
                pexp(MFMA(kf, q0, zero), m0, pa, pb); o0 = MFMA(v0, pa, o0); o0 = MFMA(v1, pb, o0);
                SBAR();
                pexp(MFMA(kf, q1, zero), m1, pa, pb); o1 = MFMA(v0, pa, o1); o1 = MFMA(v1, pb, o1);
                ka += 1024; if ((i & 3) == 3) va += vstep;
            }
#pragma unroll
            for (int i = 0; i < 9; ++i) xo[i * 64 + lane] = kh ? o0[i] : o1[i];
            LBAR();
#pragma unroll
            for (int i = 0; i < 9; ++i) o[i] = (kh ? o1[i] : o0[i]) + xi[i * 64 + lane];
        }
        const float inv = __builtin_amdgcn_rcpf(o[8]);
        st16(ws, l16, odst, (u32x4){pk(o[0] * inv, o[1] * inv), pk(o[2] * inv, o[3] * inv), pk(o[4] * inv, o[5] * inv), pk(o[6] * inv, o[7] * inv)});
    }
    if (b == 0 && w == 0) publish(fl + FL_E, tag, lane);
    STAMP(6); STAMPM(2, 3);
    };
    if (w >= 4) {
        f32x16 o0 = {}, o1 = {};
        bf16x8 q0, q1;
        u32x4 stash[32];
        u64 tag;
        f32x4 csA, csB;
        const int l = w - 4;
        u32x4 jr[2][4];
        WAITBAR(7); STAMPM(1, 2); issue(4); issue(5); issue(6); issue(7); STAMPM(1, 3);
        WAITBAR(12); STAMPM(1, 4); issue(8); issue(9); STAMPM(1, 5);
        WAITBAR(12); STAMPM(1, 6); issue(10); issue(11);
        WAITBAR(12); STAMPM(1, 7); issue(12); issue(13);
        WAITBAR(12); STAMPM(1, 8); issue(14); issue(15); STAMPM(1, 9);
        int bj = b;
        asm volatile("" : "+s"(bj));
        const int krow = lane >> 3, ch = lane & 7;
        const bool isg = bj < 112, isd = bj < 224;
        const int j0 = (isd ? bj - 112 : bj - 224) * 8 + 2 * l;
        const int nks = (isd && !isg) ? 112 : 32;
        const int lc = isg ? bj >> 2 : (isd ? j0 / 112 : j0 >> 5), ks0 = isg ? (bj & 3) * 8 + 2 * l : j0 - lc * nks;
        const unsigned rstride = isg ? 3584 : 1024;
        const unsigned jo = (ks0 * 16 + krow) * rstride + lc * 128 + ch * 16;
        const char* jsrc = isg ? wg : (isd ? wd : wo);
        const char* jsrc2 = isg ? wu : jsrc;
        const unsigned jbase = isg ? WS_WGU + lc * 4 * 32768 : (isd ? WS_WD : WS_WO) + lc * 2 * nks * 1024;
        const bool perm = isd && !isg;
        const unsigned jdst0 = jbase + (perm ? (ks0 % 7) * 16 + ks0 / 7 : ks0) * 1024, jdst1 = jbase + (perm ? ((ks0 + 1) % 7) * 16 + (ks0 + 1) / 7 : ks0 + 1) * 1024;
        const unsigned jvo = isg ? l16 : WS_END;
#pragma unroll
            for (int j = 0; j < 2; ++j) {
                jr[j][0] = *(const u32x4*)(jsrc + jo + j * 16 * rstride); jr[j][1] = *(const u32x4*)(jsrc + jo + (j * 16 + 8) * rstride);
                jr[j][2] = *(const u32x4*)(jsrc2 + jo + j * 16 * rstride); jr[j][3] = *(const u32x4*)(jsrc2 + jo + (j * 16 + 8) * rstride);
            }
        STAMPM(1, 10);
        WAITBAR(20); STAMPM(1, 11); WAITBAR(14); STAMPM(1, 12); WAITBAR(8); STAMPM(1, 13);
            LBAR();
        STAMP(1); STAMPM(1, 14);
        VMCLR();
        PIN(e_raw);
        tag = ((u64)(unsigned)rfl((int)(e_raw >> 32)) << 32 | (unsigned)rfl((int)e_raw)) + 1;
        if (l == 1) {
            const unsigned v = ((lane & 3) == 0) ? ONES2 : 0u;
            *(LAS u32x4*)(lds + M_CONST + (lane >> 3) * 512 + (lane & 7) * 16) = (u32x4){v, v, v, v};
            if (lane == 0) *(LAS unsigned*)(lds + M_WORD) = 0u;
        }
        u32x4 tf[8];
        f32x16 csa = {};
        {
            lptr st = lds + M_STG + l * 2048;
            lptr ta = st + (4 * h + ((lane & 15) >> 2)) * 64 + ((lane >> 4) & 1) * 32 + (lane & 3) * 8;
            if (isg) {
                lptr sw = st + krow * 64 + ((ch >> 1) & 1) * 1024 + (ch & 1) * 16;
#pragma unroll
                for (int i = 0; i < 4; ++i) {
                    const int j = i >> 1, rr = i & 1;
                    if ((ch >> 2) == rr) {
                        *(LAS u32x4*)sw = jr[j][0]; *(LAS u32x4*)(sw + 512) = jr[j][1];
                        *(LAS u32x4*)(sw + 32) = jr[j][2]; *(LAS u32x4*)(sw + 512 + 32) = jr[j][3];
                    }
#pragma unroll
                    for (int k = 0; k < 2; ++k) {
                        const bf16x8 f = cat(ldstr(ta + k * 1024), ldstr(ta + k * 1024 + 512));
                        const unsigned sel = (r == 2 * rr + k) ? ONES2 : 0u;
                        csa = MFMA(BC8(((u32x4){sel, sel, sel, sel})), f, csa);
                        tf[j * 4 + 2 * rr + k] = __builtin_bit_cast(u32x4, f);
                    }
                }
            } else {
                lptr sw = st + krow * 64 + (ch >> 2) * 1024 + (ch & 3) * 16;
#pragma unroll
                for (int j = 0; j < 2; ++j) {
                    *(LAS u32x4*)sw = jr[j][0]; *(LAS u32x4*)(sw + 512) = jr[j][1];
#pragma unroll
                    for (int k = 0; k < 2; ++k) {
                        tf[j * 4 + k] = __builtin_bit_cast(u32x4, cat(ldstr(ta + k * 1024), ldstr(ta + k * 1024 + 512)));
                        tf[j * 4 + 2 + k] = tf[j * 4 + k];
                    }
                }
            }
        }
        const u32x4 csv = {__float_as_uint(csa[0]), __float_as_uint(csa[1]), __float_as_uint(csa[2]), __float_as_uint(csa[3])};
        if (l == 0) {
            __builtin_amdgcn_s_sleep(SL1);
            poll(fl + FL_1 + (head >> 2) * 128 + qb * 16, 16, tag, lane);
            STAMP(4);
            gld8(ksrc - (WS_K - WS_Q) + qb * 8192, l16, lb + M_Q);
            gld4(ksrc + qb * 8192, l16, lb + S0); gld4(ksrc + qb * 8192 + 4096, l16, lb + S1);
            gld4(ksrc + (WS_V - WS_K) + qb * 8192, l16, lb + S2); gld4(ksrc + (WS_V - WS_K) + qb * 8192 + 4096, l16, lb + S3);
        }
        u32x4 cp[8];
        u64 fa, fc;
        WAITBAR(0);
        STAMP(3);
        q0 = lds128(lds + M_Q + qp * 2048 + l16); q1 = lds128(lds + M_Q + qp * 2048 + 1024 + l16);
        const unsigned sdo = WS_WD + (cb * 2 * 112 + 4 * l) * 1024;
        GAS u64* fra = fl + FL_R + 4 * (112 + 14 * cb) + (lane < 56 ? lane : 55);
        GAS u64* frc = fl + FL_R + 16 * ((7 * j4) >> 2) + (lane < 16 * (((7 * j4 + 6) >> 2) - ((7 * j4) >> 2) + 1) ? lane : 0);
        attn_main(lds, ka0, va0, vstep, q0, q1, o0, o1, [&](int c, int i) {
            const int n = c * 8 + i;
            if (n >= 1 && n < 9) {
                const int m = n - 1, k = m & 3;
                st16(ws, k < 2 ? l16 : jvo, ((m >> 2) ? jdst1 : jdst0) + k * nks * 1024, tf[m]);
            }
            if (n == 9) st16(ws, (isg && h == 0) ? r * 16 : WS_END, WS_CS + (((b & 3) * 4 + l) * 28 + lc) * 512, csv);
            if (n >= 12 && n < 36 && (n & 1) == 0) {
                const int m = (n - 12) >> 1;
                if (m < 2) st16(ws, l16, WS_O + ((qb * 8 + 2 * l + m) * 32 + head) * 1024, poison);
                else if (m < 4) st16(ws, l16, WS_H + ((rb * 2 + (l & 1)) * 32 + cb * 4 + (l >> 1) * 2 + m - 2) * 1024, poison);
                else if (m == 4) st16(ws, r * 16, WS_HS + ((rb * 2 + (l & 1)) * 8 + cb) * 512, poison);
                else st16(ws, l16, WS_ACT + ((g4 * 4 + l) * 112 + j4 * 7 + m - 5) * 1024, poison);
            }
            if (n == 25) { fa = __hip_atomic_load(fra, RLX); fc = __hip_atomic_load(frc, RLX); }
            if (n >= 29 && n < 61 && (n & 1) == 1) {
                const int m = (n - 29) >> 1;
                stash[2 * m] = ld16(ws, l16, sdo + ((m >> 2) * 16 + (m & 3)) * 1024);
                stash[2 * m + 1] = ld16(ws, l16, sdo + (112 + (m >> 2) * 16 + (m & 3)) * 1024);
            }
            if (n == 28) {
#pragma unroll
                for (int k = 0; k < 4; ++k) {
                    const unsigned so = WS_CS + ((4 * l + k) * 28 + ((7 * j4) >> 2)) * 512;
                    cp[k] = ld16(ws, l16, so); cp[4 + k] = ld16(ws, r * 16, so + 1024);
                }
            }
        }, [&](int c, int i, auto settle) {
            const int n = c * 8 + i;
            if (n == 6 || n == 14) {
                BAR();
            } else if (n == 12) {
                settle();
                VMWAIT(0);
                publish(fl + FL_R + b * 4 + l, tag, lane);
            } else if (n == 28) {
                settle();
                PIN(fa); PIN(fc);
                if (!__all(fa == tag && fc == tag)) { RCINC(6, 1); poll(fra, tag); poll(frc, tag); }
            } else if (n == 32) {
                asm volatile("" : "+v"(cp[0]), "+v"(cp[1]), "+v"(cp[2]), "+v"(cp[3]), "+v"(cp[4]), "+v"(cp[5]), "+v"(cp[6]), "+v"(cp[7]));
                csA = (__builtin_bit_cast(f32x4, cp[0]) + __builtin_bit_cast(f32x4, cp[1])) + (__builtin_bit_cast(f32x4, cp[2]) + __builtin_bit_cast(f32x4, cp[3]));
                csB = (__builtin_bit_cast(f32x4, cp[4]) + __builtin_bit_cast(f32x4, cp[5])) + (__builtin_bit_cast(f32x4, cp[6]) + __builtin_bit_cast(f32x4, cp[7]));
                PIN(csA); PIN(csB);
            } else if (n == 38) {
                settle();
                STAMP(8);
                VMWAIT(4);
                STAMP(9);
                publish(fl + FL_P + b * 4 + l, tag, lane);
            }
        });
        attn_finish(o0, o1, q0, q1, tag);
        const char* gu = ws_ + WS_WGU + (size_t)j4 * 7 * 32768 + l * 8192;
        const char* wdn = ws_ + WS_WD + (size_t)((cb * 2 + (l >> 1)) * 112 + 80) * 1024 + (l & 1) * 8192;
        const unsigned lq = lb + l * 8192;
        {
            gld8(gu, l16, lq + S0);
            STAMPM(2, 4);
            __builtin_amdgcn_s_sleep(SL2);
            STAMP(7);
            const char* os = ws_ + WS_O + (size_t)(rb * 2 + (l >> 1)) * 32768 + (l & 1) * 4096;
            const unsigned lo = lb + S3 + l * 4096;
            gld4(os, l16, lo); gld4(os + 8192, l16, lo + 16384);
            STAMPM(2, 5);
            WAITBAR(4);
            STAMPM(2, 6);
            gld4(os + 16384, l16, lo + 32768); gld4(os + 24576, l16, lo + 49152);
            publish(myf2, tag, lane);
            STAMPM(2, 7);
            WAITBAR(9); STAMPM(2, 8); gld8(gu + 32768, l16, lq + S1); STAMPM(2, 9);
            WAITBAR(13);
            WAITBAR(9);
        }
        LBAR();
        STAMPM(2, 10);
        gld8(gu + 2 * 32768, l16, lq + S2); gld8(gu + 3 * 32768, l16, lq + S3);
        STAMPM(2, 11);
        if (l == 0 && lane == 0) *(LAS unsigned*)(lds + M_CNT) = 0u;
        *(LAS f32x4*)(lds + M0 + 1024 + 2048 * l + l16) = csA; *(LAS f32x4*)(lds + M0 + 1024 + 2048 * l + 1024 + l16) = csB;
        STAMPM(2, 12);
        WAITBAR(8);
        {
            const int ft = ((7 * j4) & 3) + 2 * l + h;
            LAS float* p = (LAS float*)(lds + M0 + 1024 + (ft >> 2) * 512 + r * 16 + (ft & 3) * 4);
            *(LAS float*)(lds + M0 + (l * 64 + lane) * 4) = (p[0] + p[512]) + (p[1024] + p[1536]);
        }
        WAITBAR(0);
        STAMPM(3, 1);
        gld8(gu + 4 * 32768, l16, lq + S0); gld8(gu + 5 * 32768, l16, lq + S1); gld8(gu + 6 * 32768, l16, lq + S2);
        STAMPM(3, 2);
        WAITBAR(16); STAMPM(3, 3); gld8(wdn, l16, lq + S3); STAMPM(3, 4);
        WAITBAR(16); STAMPM(3, 5); gld8(wdn + 16384, l16, lq + S0); STAMPM(3, 6);
        WAITBAR(0); STAMPM(3, 7);
        f32x16 C[4];
        u32x4 af[20][2], wx[8];
        constexpr int AH = 6;
        auto aoff = [&](int k, int t) { return WS_ACT + ((rb * 2 + t) * 112 + (4 * l + (k & 3)) * 7 + (k >> 2)) * 1024; };
#pragma unroll
        for (int k = 0; k < AH; ++k) { af[k][0] = ld16(ws, l16, aoff(k, 0)); af[k][1] = ld16(ws, l16, aoff(k, 1)); }
        SBAR();
#pragma unroll
        for (int k = 0; k < 20; ++k) {
            if (k == 0) STAMPM(3, 9);
            if ((k & 1) == 0) verify4(af[k][0], af[k][1], af[k + 1][0], af[k + 1][1], ws, l16, aoff(k, 0), aoff(k, 1), aoff(k + 1, 0), aoff(k + 1, 1), fl + FL_4 + rb * 32, 32, tag RC(5));
            if (k == 0) STAMPM(3, 10); if (k == 4) STAMPM(3, 11); if (k == 8) STAMPM(3, 12); if (k == 16) STAMPM(3, 13);
#pragma unroll
            for (int q = 0; q < 4; ++q) C[q] = MFMA(BC8(k < 16 ? stash[2 * k + (q >> 1)] : wx[2 * (k - 16) + (q >> 1)]), BC8(af[k][q & 1]), k ? C[q] : f32x16{});
            SBAR();
            if (k + AH < 20) { af[k + AH][0] = ld16(ws, l16, aoff(k + AH, 0)); af[k + AH][1] = ld16(ws, l16, aoff(k + AH, 1)); }
            if (k >= 6 && k < 10) {
                const unsigned wo4 = WS_WD + (cb * 2 * 112 + 64 + 4 * l + k - 6) * 1024;
                wx[2 * (k - 6)] = ld16(ws, l16, wo4); wx[2 * (k - 6) + 1] = ld16(ws, l16, wo4 + 112 * 1024);
            }
            SBAR();
        }
        STAMP(12); STAMPM(3, 14);
        if (l >= 2) {
#pragma nounroll
            for (int t = 0; t < (1 << 20); ++t) {
                if (rfl(__hip_atomic_load((LAS unsigned*)(lds + M_CNT), __ATOMIC_RELAXED, __HIP_MEMORY_SCOPE_WORKGROUP)) == 4) break;
                __builtin_amdgcn_s_sleep(1);
            }
            asm volatile("" ::: "memory");
        }
        lptr pp = lds + opaque(S1 + l * 16384 + l16);
#pragma unroll
        for (int q = 0; q < 4; ++q)
#pragma unroll
            for (int g = 0; g < 4; ++g) *(LAS f32x4*)(pp + q * 4096 + g * 1024) = (f32x4){C[q][4 * g], C[q][4 * g + 1], C[q][4 * g + 2], C[q][4 * g + 3]};
        STAMP(14);
        LBAR();
    } else {
        f32x16 o0 = {}, o1 = {};
        bf16x8 q0, q1;
        u32x4 stash[32];
        u64 tag;
        if (w >> 1) p1_consume<1>(lds, ws, lane, w & 1, start1, rb1, hq); else p1_consume<0>(lds, ws, lane, w & 1, start1, rb1, hq);
        STAMP(1); STAMPM(1, 14);
        LBAR();
        VMWAIT(0);
        PIN(e_raw);
        tag = ((u64)(unsigned)rfl((int)(e_raw >> 32)) << 32 | (unsigned)rfl((int)e_raw)) + 1;
        publish(fl + FL_1 + (hq * 32 + rb1) * 4 + w, tag, lane);
        STAMP(2);
        LBAR();
        STAMP(3);
        q0 = lds128(lds + M_Q + qp * 2048 + l16); q1 = lds128(lds + M_Q + qp * 2048 + 1024 + l16);
        const char* dsrc = ksrc + (w >> 1) * (WS_V - WS_K) + (w & 1) * 4096;
        const unsigned ddst = lb + w * 32768;
        const unsigned swo = WS_WO + (cb * 2 + (w >> 1)) * 32768;
        GAS u64* frb = fl + FL_R + 4 * (224 + 4 * cb) + (lane & 15);
        u32x4 f1, fp[2];
        u64 fb;
        attn_main(lds, ka0, va0, vstep, q0, q1, o0, o1, [&](int c, int i) {
            const int n = c * 8 + i;
            if (n == 0) f1 = ld16(ws, l16, WS_FL + (FL_1 + (head >> 2) * 128) * 8);
            if (n >= 2 && n < 9) gld4(dsrc + ((qb + n - 1) & 7) * 8192, l16, ddst + (n - 1) * 4096);
            if (n == 25) fb = __hip_atomic_load(frb, RLX);
            if (n >= 29 && n < 61 && (n & 1) == 1) {
                stash[n - 29] = ld16(ws, l16, swo + (n - 29) * 1024);
                stash[n - 28] = ld16(ws, l16, swo + (n - 28) * 1024);
            }
            if (n == 56) { fp[0] = ld16(ws, l16, WS_FL + FL_P * 8 + w * 2048); fp[1] = ld16(ws, l16, WS_FL + FL_P * 8 + w * 2048 + 1024); }
        }, [&](int c, int i, auto settle) {
            const int n = c * 8 + i;
            if (n == 2) {
                settle();
                PIN(f1);
                if (!__all(tags2(f1, tag))) { RCINC(0, 1); poll128(fl + FL_1 + (head >> 2) * 128, tag, lane); }
            } else if (n == 6) { VMWAIT(12); BAR(); }
            else if (n == 14) { VMWAIT(0); BAR(); }
            else if (n == 28) {
                settle();
                PIN(fb);
                if (!__all(fb == tag)) { RCINC(6, 1); poll(frb, tag); }
            } else if (n == 60) {
                settle();
                PIN(fp[0]); PIN(fp[1]);
                if (!__all(tags2(fp[0], tag) && tags2(fp[1], tag))) { RCINC(7, 1); poll128(fl + FL_P + w * 256, tag, lane); poll128(fl + FL_P + w * 256 + 128, tag, lane); }
            }
        });
        attn_finish(o0, o1, q0, q1, tag);
        const int tt = w & 1, ct = w >> 1;
        const int ttile = rb * 2 + tt, ctile = cb * 2 + ct;
        u32x2 rx[4];
        {
            const char* p = x + (size_t)(ttile * 32 + r) * 1024 + (ctile * 32 + 4 * h) * 2;
#pragma unroll
            for (int g = 0; g < 4; ++g) rx[g] = *(const u32x2*)(p + g * 16);
        }
        u32x4 hr[2];
        {
            f32x16 acc = {};
            lptr ba = lds + opaque(S3 + tt * 8192 + l16);
            VMWAIT(4);
            publish(myf2, tag, lane);
            STAMPM(2, 4);
#pragma unroll
            for (int qd = 0; qd < 4; ++qd) {
                LBAR();
                STAMPM(2, 5 + qd);
                if (qd == 0) STAMP(8);
                u32x4 bf[8];
#pragma unroll
                for (int ks = 0; ks < 8; ++ks) bf[ks] = *(LAS u32x4*)(ba + qd * 16384 + ks * 1024);
                SBAR();
#pragma unroll
                for (int g = 0; g < 2; ++g) {
                    const unsigned oo = WS_O + (ttile * 32 + qd * 8 + g * 4) * 1024;
                    verify4(bf[4 * g], bf[4 * g + 1], bf[4 * g + 2], bf[4 * g + 3], ws, l16, oo, oo + 1024, oo + 2048, oo + 3072, fl + FL_2 + ttile * 32, 32, tag RC(2));
#pragma unroll
                    for (int ks = 4 * g; ks < 4 * g + 4; ++ks) acc = MFMA(BC8(stash[qd * 8 + ks]), BC8(bf[ks]), acc);
                }
            }
#pragma unroll
            for (int g = 0; g < 4; ++g) {
                hr[g >> 1][(g & 1) * 2] = pk(acc[4 * g] + bflo(rx[g][0]), acc[4 * g + 1] + bfhi(rx[g][0]));
                hr[g >> 1][(g & 1) * 2 + 1] = pk(acc[4 * g + 2] + bflo(rx[g][1]), acc[4 * g + 3] + bfhi(rx[g][1]));
            }
            st16(ws, l16, WS_H + (ttile * 32 + ctile * 2) * 1024, hr[0]);
            st16(ws, l16, WS_H + (ttile * 32 + ctile * 2 + 1) * 1024, hr[1]);
            float s1 = 0.f, s2 = 0.f;
            stat2(hr[0], s1, s2); stat2(hr[1], s1, s2);
            s1 = halfsum(s1); s2 = halfsum(s2);
            if (h == 0) __hip_atomic_store((GAS u64*)(ws_ + WS_HS) + ((ttile * 8 + cb) * 32 + r) * 2 + ct, (u64)__float_as_uint(s1) | ((u64)__float_as_uint(s2) << 32), RLX);
        }
        STAMPM(2, 9);
        LBAR();
        STAMP(9); STAMPM(2, 10);
        const int t4 = g4 * 4 + w;
        __builtin_amdgcn_s_sleep(SL3);
        u32x4 hs[4], hf[32];
        constexpr int HAHEAD = 12;
        const unsigned ho = WS_H + t4 * 32768;
#pragma unroll
        for (int i = 0; i < 4; ++i) hs[i] = ld16(ws, l16 + i * 1024, WS_HS + t4 * 4096);
#pragma unroll
        for (int ks = 0; ks < HAHEAD; ++ks) hf[ks] = ld16(ws, l16, ho + ks * 1024);
        STAMPM(2, 11);
        VMWAIT(4 + HAHEAD);
        STAMPM(2, 12);
        publish(fl + FL_3 + ttile * 16 + ctile, tag, lane);
        LBAR();
        STAMPM(2, 13);
        { const unsigned so = WS_HS + t4 * 4096; verify4(hs[0], hs[1], hs[2], hs[3], ws, l16, so, so + 1024, so + 2048, so + 3072, fl + FL_3 + t4 * 16, 16, tag RC(3)); }
        STAMP(10); STAMPM(2, 14);
        float s1 = 0.f, s2 = 0.f;
#pragma unroll
        for (int i = 0; i < 4; ++i) { s1 += __uint_as_float(hs[i][0]) + __uint_as_float(hs[i][2]); s2 += __uint_as_float(hs[i][1]) + __uint_as_float(hs[i][3]); }
        s1 = halfsum(s1); s2 = halfsum(s2);
        const float m = s1 * (1.f / D);
        const float vv = fmaxf(s2 * (1.f / D) - m * m, 0.f) + 1e-5f;
        const float rs = __builtin_amdgcn_rsqf(vv);
        constexpr float C1 = -2.f * 0.7978845608028654f * 1.4426950408889634f, C2 = C1 * 0.044715f;
        const float k1 = C1 * rs, k2 = C2 * rs * rs * rs;
        SBAR();
        f32x16 A[4];
        lptr wlo = lds + opaque(l16), whi = lds + opaque(S2 + l16);
        {
            bf16x8 wf[2][3];
            wf[0][0] = lds128(wlo); wf[0][1] = lds128(wlo + S1); wf[0][2] = lds128(whi);
            SBAR();
#pragma unroll
            for (int ks = 0; ks < 32; ++ks) {
                const int c = ks & 1, n = c ^ 1;
                if ((ks & 3) == 0) verify4(hf[ks], hf[ks + 1], hf[ks + 2], hf[ks + 3], ws, l16, ho + ks * 1024, ho + (ks + 1) * 1024, ho + (ks + 2) * 1024, ho + (ks + 3) * 1024, fl + FL_3 + t4 * 16, 16, tag RC(4));
                if (ks + HAHEAD < 32) hf[ks + HAHEAD] = ld16(ws, l16, ho + (ks + HAHEAD) * 1024);
                if (ks < 31) wf[n][0] = lds128(wlo + (ks + 1) * 1024);
                SBAR(); A[0] = MFMA(wf[c][0], BC8(hf[ks]), ks ? A[0] : f32x16{}); SBAR();
                if (ks < 31) wf[n][1] = lds128(wlo + S1 + (ks + 1) * 1024);
                SBAR(); A[1] = MFMA(wf[c][1], BC8(hf[ks]), ks ? A[1] : f32x16{}); SBAR();
                if (ks < 31) wf[n][2] = lds128(whi + (ks + 1) * 1024);
                SBAR(); A[2] = MFMA(wf[c][2], BC8(hf[ks]), ks ? A[2] : f32x16{}); SBAR();
            }
        }
        LAS float* cs = (LAS float*)(lds + opaque(M0 + 16 * h));
        const unsigned adst = WS_ACT + (t4 * 112 + j4 * 7) * 1024;
        float ea[4], eu[4], et[4];
        f32x4 cg, cu;
        u32x4 eo;
        auto epi = [&](auto G_) __attribute__((always_inline)) {
            constexpr int G = decltype(G_)::v;
            constexpr int hf_ = G / 13, st = G % 13, t = hf_ >> 1, e0 = (hf_ & 1) * 4;

            if constexpr (st == 0) {
#pragma unroll
                for (int e = 0; e < 4; ++e) { ea[e] = fmaf(-m, cg[e], A[t & 3][e0 + e]); PIN(ea[e]); }
            } else if constexpr (st == 1) {
#pragma unroll
                for (int e = 0; e < 4; ++e) { eu[e] = fmaf(-m, cu[e], A[t & 3][8 + e0 + e]); PIN(eu[e]); }
            } else if constexpr (st == 2) {
#pragma unroll
                for (int e = 0; e < 4; ++e) { et[e] = ea[e] * ea[e]; PIN(et[e]); }
            } else if constexpr (st == 3) {
#pragma unroll
                for (int e = 0; e < 4; ++e) { et[e] = fmaf(k2, et[e], k1); PIN(et[e]); }
            } else if constexpr (st == 4) {
#pragma unroll
                for (int e = 0; e < 4; ++e) { et[e] *= ea[e]; PIN(et[e]); }
            } else if constexpr (st == 5) {
#pragma unroll
                for (int e = 0; e < 4; ++e) { eu[e] *= ea[e]; PIN(eu[e]); }
            } else if constexpr (st == 6 || st == 7) {
#pragma unroll
                for (int e = 2 * (st - 6); e < 2 * (st - 6) + 2; ++e) { et[e] = __builtin_amdgcn_exp2f(et[e]); PIN(et[e]); }
                if constexpr (hf_ + 1 < 14) {
                    LAS float* c = cs + ((hf_ + 1) >> 1) * 32 + ((hf_ + 1) & 1) * 8;
                    if (st == 6) cg = *(LAS f32x4*)c; else cu = *(LAS f32x4*)(c + 16);
                }
            } else if constexpr (st == 8) {
#pragma unroll
                for (int e = 0; e < 4; ++e) { et[e] = fmaf(et[e], vv, vv); PIN(et[e]); }
            } else if constexpr (st == 9 || st == 10) {
#pragma unroll
                for (int e = 2 * (st - 9); e < 2 * (st - 9) + 2; ++e) { et[e] = __builtin_amdgcn_rcpf(et[e]); PIN(et[e]); }
            } else if constexpr (st == 11) {
#pragma unroll
                for (int e = 0; e < 4; ++e) { eu[e] *= et[e]; PIN(eu[e]); }
            } else {
                eo[e0 >> 1] = pk(eu[0], eu[1]); eo[(e0 >> 1) + 1] = pk(eu[2], eu[3]);
                if (e0) st16(ws, l16, adst + t * 1024, eo); else { PIN(eo[0]); PIN(eo[1]); }
            }
        };
        auto ftile = [&](auto t_, lptr p) __attribute__((always_inline)) {
            constexpr int t = decltype(t_)::v;
            bf16x8 wf[4];
            wf[0] = lds128(p); wf[1] = lds128(p + 1024); wf[2] = lds128(p + 2048);
            SBAR();
            sfor<0, 32>([&](auto ks_) __attribute__((always_inline)) {
                constexpr int ks = decltype(ks_)::v;
                if (ks + 3 < 32) wf[(ks + 3) & 3] = lds128(p + (ks + 3) * 1024);
                epi(IC<(t - 3) * 32 + ks>{});
                SBAR();
                A[t & 3] = MFMA(wf[ks & 3], BC8(hf[ks]), ks ? A[t & 3] : f32x16{});
                SBAR();
            });
        };
        STAMPM(2, 15);
        LBAR();
        cg = *(LAS f32x4*)cs; cu = *(LAS f32x4*)(cs + 16);
        STAMP(11); STAMPM(3, 1);
        ftile(IC<3>{}, whi + 32768);
        STAMPM(3, 2);
        LBAR();
        STAMPM(3, 3);
        ftile(IC<4>{}, wlo);
        STAMPM(3, 4);
        LBAR();
        STAMPM(3, 5);
        ftile(IC<5>{}, wlo + S1);
        STAMPM(3, 6);
        LBAR();
        STAMPM(3, 7);
        ftile(IC<6>{}, whi);
        STAMPM(3, 8);
        asm volatile("s_waitcnt lgkmcnt(0)" ::: "memory");
        if (lane == 0) __hip_atomic_fetch_add((LAS unsigned*)(lds + M_CNT), 1u, __ATOMIC_RELAXED, __HIP_MEMORY_SCOPE_WORKGROUP);
        sfor<128, 14 * 13>(epi);
        STAMPM(3, 9);
        STAMP(12);
        f32x16 C[4];
        {
            u32x4 af[8][2];
            auto aoff = [&](int k, int t) { return WS_ACT + ((rb * 2 + t) * 112 + (4 * w + (k & 3)) * 7 + 5 + (k >> 2)) * 1024; };
#pragma unroll
            for (int k = 0; k < 8; ++k) { af[k][0] = ld16(ws, l16, aoff(k, 0)); af[k][1] = ld16(ws, l16, aoff(k, 1)); }
            lptr wa0 = lds + opaque(w * 4096 + l16), wa3 = lds + opaque(S3 + w * 4096 + l16);
            bf16x8 wf[8][2];
#pragma unroll
            for (int k = 0; k < 8; ++k) { lptr p = (k < 4 ? wa3 : wa0) + (k & 3) * 1024; wf[k][0] = lds128(p); wf[k][1] = lds128(p + 16384); }
            STAMPM(3, 10);
            VMWAIT(16);
            STAMPM(3, 11);
            publish(fl + FL_4 + t4 * 16 + j4, tag, lane);
            STAMP(13); STAMPM(3, 12);
#pragma unroll
            for (int k = 0; k < 8; ++k) {
                if ((k & 1) == 0) verify4(af[k][0], af[k][1], af[k + 1][0], af[k + 1][1], ws, l16, aoff(k, 0), aoff(k, 1), aoff(k + 1, 0), aoff(k + 1, 1), fl + FL_4 + rb * 32, 32, tag RC(5));
#pragma unroll
                for (int q = 0; q < 4; ++q) C[q] = MFMA(wf[k][q >> 1], BC8(af[k][q & 1]), k ? C[q] : f32x16{});
                SBAR();
            }
            STAMPM(3, 13);
#pragma unroll
            for (int q = 0; q < 4; ++q) {
                if (q != w) {
                    const int pc = (q - w - 1) & 3;
                    lptr p = pc == 0 ? wa3 : (pc == 1 ? wa3 + 16384 : wa0);
#pragma unroll
                    for (int g = 0; g < 4; ++g) *(LAS f32x4*)(p + g * 1024) = (f32x4){C[q][4 * g], C[q][4 * g + 1], C[q][4 * g + 2], C[q][4 * g + 3]};
                }
            }
        }
        f32x16 acc = w == 0 ? C[0] : (w == 1 ? C[1] : (w == 2 ? C[2] : C[3]));
        STAMPM(3, 14);
        LBAR();
        STAMPM(3, 15);
        {
            lptr lp1 = lds + opaque(S1 + w * 4096 + l16);
#pragma unroll
            for (int s = 0; s < 7; ++s) {
                lptr p;
                if (s < 4) p = lp1 + s * 16384;
                else { const int pc = s - 4, src_w = (w - pc - 1) & 3; p = lds + opaque(src_w * 4096 + l16 + (pc == 0 ? S3 : (pc == 1 ? S3 + 16384 : 0))); }
#pragma unroll
                for (int g = 0; g < 4; ++g) {
                    const f32x4 v = *(LAS f32x4*)(p + g * 1024);
#pragma unroll
                    for (int j = 0; j < 4; ++j) acc[4 * g + j] += v[j];
                }
            }
        }
        STAMP(14);
        char* p = out + (size_t)(ttile * 32 + r) * 1024 + (ctile * 32 + 4 * h) * 2;
#pragma unroll
        for (int g = 0; g < 4; ++g) {
            const unsigned a = hr[g >> 1][(g & 1) * 2], c = hr[g >> 1][(g & 1) * 2 + 1];
            *(u32x2*)(p + g * 16) = (u32x2){pk(acc[4 * g] + bflo(a), acc[4 * g + 1] + bfhi(a)), pk(acc[4 * g + 2] + bflo(c), acc[4 * g + 3] + bfhi(c))};
        }
    }
    STAMP(15); STAMP_FLUSH(ws_);
}

extern "C" void kernel_launch(void* const* d_in, const int* in_sizes, int n_in, void* d_out, int out_size, void* d_ws, size_t ws_size,
                              hipStream_t stream) {
    (void)hipFuncSetAttribute((const void*)fused, hipFuncAttributeMaxDynamicSharedMemorySize, LDS_BYTES);
    fused<<<256, 512, LDS_BYTES, stream>>>((const char*)d_in[0], (const char*)d_in[1], (const char*)d_in[2], (const char*)d_in[4],
                                           (const char*)d_in[5], (const char*)d_in[6], (char*)d_out, (char*)d_ws);
}
```

```cpp
#include <hip/hip_runtime.h>
#include <hip/hip_bf16.h>
#include <stdint.h>

typedef __attribute__((ext_vector_type(8))) short bf16x8;
typedef __attribute__((ext_vector_type(4))) short s16x4;
typedef __attribute__((ext_vector_type(16))) float f32x16;
typedef __attribute__((ext_vector_type(4))) float f32x4;
typedef __attribute__((ext_vector_type(2))) float f32x2;
typedef __attribute__((ext_vector_type(4))) unsigned u32x4;
typedef __attribute__((ext_vector_type(2))) unsigned u32x2;
typedef __attribute__((ext_vector_type(2))) __bf16 bf16x2;
typedef unsigned long long u64;
#define LAS __attribute__((address_space(3)))
#define GAS __attribute__((address_space(1)))
typedef LAS char* lptr;

#define MFMA(a, b, c) __builtin_amdgcn_mfma_f32_32x32x16_bf16(a, b, c, 0, 0, 0)
#define SBAR() __builtin_amdgcn_sched_barrier(0)
#define WAITBAR(N) asm volatile("s_waitcnt vmcnt(%0) lgkmcnt(0)\n\ts_barrier" :: "i"(N) : "memory")
#define LBAR() asm volatile("s_waitcnt lgkmcnt(0)\n\ts_barrier" ::: "memory")
#define VMWAIT(N) asm volatile("s_waitcnt vmcnt(%0)" :: "i"(N) : "memory")
#define VMCLR() __builtin_amdgcn_s_waitcnt(0x0F70)
#define RLX __ATOMIC_RELAXED, __HIP_MEMORY_SCOPE_AGENT

#define STAMP_DECL do {} while (0)
#define RCARG
#define RC(i)
#define RCADD(n) do {} while (0)
#define RCINC(i, n) do {} while (0)
#define STAMP(idx) do {} while (0)
#define STAMPM(m, idx) do {} while (0)
#define STAMP_FLUSH(ws) do {} while (0)

constexpr int D = 512;
constexpr unsigned WS_Q = 0;
constexpr unsigned WS_K = WS_Q + (2u << 20);
constexpr unsigned WS_V = WS_K + (2u << 20);
constexpr unsigned WS_O = WS_V + (2u << 20);
constexpr unsigned WS_H = WS_O + (2u << 20);
constexpr unsigned WS_ACT = WS_H + (2u << 20);
constexpr unsigned WS_WO = WS_ACT + (7u << 20);
constexpr unsigned WS_WGU = WS_WO + (512u << 10);
constexpr unsigned WS_WD = WS_WGU + (3584u << 10);
constexpr unsigned WS_HS = WS_WD + (1792u << 10);
constexpr unsigned WS_CS = WS_HS + (256u << 10);
constexpr unsigned WS_FL = WS_CS + (256u << 10);
constexpr unsigned WS_END = WS_FL + 65536;
constexpr int FL_P = 0, FL_1 = 1024, FL_R = 2048, FL_2 = 3072, FL_3 = 5120, FL_4 = 6144, FL_E = 7168;
constexpr unsigned POISON = 0xFFFFFFFFu;
constexpr int S0 = 0, S1 = 32768, S2 = 65536, S3 = 98304, M0 = 131072, LDS_BYTES = 163840;
constexpr int M_Q = M0, M_X = M0, M_STG = M0 + 18432, M_CONST = M0 + 26624, M_WORD = M0 + 30720, M_CNT = M0 + 16384;
constexpr int P1_W = 65536, P1_SLOT = 24576;

__device__ __forceinline__ void glds3(const void* sbase, unsigned voff, unsigned ldsaddr) {
    asm volatile("s_mov_b32 m0, %2\n\ts_nop 0\n\tglobal_load_lds_dwordx4 %0, %1\n\tglobal_load_lds_dwordx4 %0, %1 offset:1024\n\tglobal_load_lds_dwordx4 %0, %1 offset:2048"
                 :: "v"(voff), "s"(sbase), "s"(ldsaddr) : "memory");
}
__device__ __forceinline__ void glds3_upper(int w, const void* sbase, unsigned voff, unsigned ldsaddr) {
    asm volatile("s_cmp_lt_u32 %3, 4\n\ts_cbranch_scc1 1f\n\ts_mov_b32 m0, %2\n\ts_nop 0\n\tglobal_load_lds_dwordx4 %0, %1\n\t"
                 "global_load_lds_dwordx4 %0, %1 offset:1024\n\tglobal_load_lds_dwordx4 %0, %1 offset:2048\n1:"
                 :: "v"(voff), "s"(sbase), "s"(ldsaddr), "s"(w) : "scc", "memory");
}
__device__ __forceinline__ void touch(unsigned& d, const char* p, int ix, int first, int n) {
    asm volatile("s_cmp_lt_u32 %2, %3\n\ts_cbranch_scc0 1f\n\ts_load_dword %0, %1, 0\n1:" : "+s"(d) : "s"(p), "s"(ix - first), "s"(n) : "scc", "memory");
}
__device__ __forceinline__ void gld2(const void* sbase, unsigned voff, unsigned ldsaddr) {
    asm volatile("s_mov_b32 m0, %2\n\ts_nop 0\n\tglobal_load_lds_dwordx4 %0, %1 sc1\n\tglobal_load_lds_dwordx4 %0, %1 offset:1024 sc1"
                 :: "v"(voff), "s"(sbase), "s"(ldsaddr) : "memory");
}
__device__ __forceinline__ void gld4(const void* sbase, unsigned voff, unsigned ldsaddr) {
    asm volatile("s_mov_b32 m0, %2\n\ts_nop 0\n\tglobal_load_lds_dwordx4 %0, %1 sc1\n\tglobal_load_lds_dwordx4 %0, %1 offset:1024 sc1\n\t"
                 "global_load_lds_dwordx4 %0, %1 offset:2048 sc1\n\tglobal_load_lds_dwordx4 %0, %1 offset:3072 sc1"
                 :: "v"(voff), "s"(sbase), "s"(ldsaddr) : "memory");
}
__device__ __forceinline__ void gld8(const char* sbase, unsigned voff, unsigned ldsaddr) {
    asm volatile("s_mov_b32 m0, %2\n\ts_nop 0\n\tglobal_load_lds_dwordx4 %0, %1 offset:-4096 sc1\n\tglobal_load_lds_dwordx4 %0, %1 offset:-3072 sc1\n\t"
                 "global_load_lds_dwordx4 %0, %1 offset:-2048 sc1\n\tglobal_load_lds_dwordx4 %0, %1 offset:-1024 sc1\n\t"
                 "global_load_lds_dwordx4 %0, %1 sc1\n\tglobal_load_lds_dwordx4 %0, %1 offset:1024 sc1\n\t"
                 "global_load_lds_dwordx4 %0, %1 offset:2048 sc1\n\tglobal_load_lds_dwordx4 %0, %1 offset:3072 sc1"
                 :: "v"(voff), "s"(sbase + 4096), "s"(ldsaddr + 4096) : "memory");
}
__device__ __forceinline__ bf16x8 lds128(lptr p) { return *(const LAS bf16x8*)p; }
__device__ __forceinline__ s16x4 ldstr(lptr p) { return __builtin_amdgcn_ds_read_tr16_b64_v4i16((LAS s16x4*)p); }
__device__ __forceinline__ bf16x8 cat(s16x4 a, s16x4 b) { return __builtin_shufflevector(a, b, 0, 1, 2, 3, 4, 5, 6, 7); }
__device__ __forceinline__ unsigned pk(float lo, float hi) { bf16x2 r = {(__bf16)lo, (__bf16)hi}; return __builtin_bit_cast(unsigned, r); }
__device__ __forceinline__ float dot2(unsigned a, unsigned b, float c) {
    return __builtin_amdgcn_fdot2_f32_bf16(__builtin_bit_cast(bf16x2, a), __builtin_bit_cast(bf16x2, b), c, false);
}
__device__ __forceinline__ float bflo(unsigned u) { return __uint_as_float(u << 16); }
__device__ __forceinline__ float bfhi(unsigned u) { return __uint_as_float(u & 0xffff0000u); }
constexpr unsigned ONES2 = 0x3F803F80u;
__device__ __forceinline__ void stat2(u32x4 u, float& s1, float& s2) {
#pragma unroll
    for (int i = 0; i < 4; ++i) { s1 = dot2(u[i], ONES2, s1); s2 = dot2(u[i], u[i], s2); }
}
__device__ __forceinline__ void stat1(bf16x8 f, float& s1) {
    u32x4 u = __builtin_bit_cast(u32x4, f);
#pragma unroll
    for (int i = 0; i < 4; ++i) s1 = dot2(u[i], ONES2, s1);
}
__device__ __forceinline__ unsigned opaque(unsigned x) { asm volatile("" : "+v"(x)); return x; }
template <int I> struct IC { static constexpr int v = I; };
template <int B, int E, class Fn> __device__ __forceinline__ void sfor(Fn&& f) { if constexpr (B < E) { f(IC<B>{}); sfor<B + 1, E>(f); } }
#define PIN(x) asm volatile("" : "+v"(x))
__device__ __forceinline__ int fresh_lane() {
    int x; asm volatile("v_mbcnt_lo_u32_b32 %0, -1, 0\n\tv_mbcnt_hi_u32_b32 %0, -1, %0" : "=v"(x)); return x;
}
__device__ __forceinline__ int rfl(int x) { return __builtin_amdgcn_readfirstlane(x); }
__device__ __forceinline__ float halfsum(float x) {
    auto r = __builtin_amdgcn_permlane32_swap(__float_as_uint(x), __float_as_uint(x), false, false);
    return __uint_as_float(r[0]) + __uint_as_float(r[1]);
}
typedef __amdgpu_buffer_rsrc_t rsrc_t;
__device__ __forceinline__ void st16(rsrc_t r, unsigned voff, unsigned soff, u32x4 v) {
    __builtin_amdgcn_raw_buffer_store_b128(v, r, voff, soff, 16);
    asm volatile("s_nop 1" :: "v"(v));
}
__device__ __forceinline__ u32x4 ld16(rsrc_t r, unsigned voff, unsigned soff) { return __builtin_amdgcn_raw_buffer_load_b128(r, voff, soff, 16); }
__device__ __forceinline__ void publish(GAS u64* f, u64 tag, int lane) { if (lane == 0) __hip_atomic_store(f, tag, RLX); }
__device__ __forceinline__ void poll(GAS u64* f, u64 tag) {
#pragma nounroll
    for (unsigned spins = 0; spins < (1u << 16); ++spins) {
        const u64 v = __hip_atomic_load(f, RLX);
        if (__all(v == tag)) break;
        __builtin_amdgcn_s_sleep(2);
    }
    asm volatile("" ::: "memory");
}
__device__ __forceinline__ void poll(GAS u64* f, int n, u64 tag, int lane) { poll(f + (lane & (n - 1)), tag); }
#define SL1 20
#define SL2 8
#define SL3 8
__device__ __forceinline__ unsigned mx(u32x4 v, unsigned m) { return max(max(v[2], v[3]), max(max(v[0], v[1]), m)); }
__device__ __forceinline__ void verify4(u32x4& a, u32x4& b, u32x4& c, u32x4& d, rsrc_t ws, unsigned voff, unsigned oa, unsigned ob, unsigned oc,
                                        unsigned od, GAS u64* flags, int n, u64 tag RCARG) {
    if (__builtin_expect(__any(mx(d, mx(c, mx(b, mx(a, 0u)))) == POISON), 0)) {
#pragma nounroll
        for (int t = 0;; ++t) {
            if (t == 32) poll(flags, n, tag, (int)(voff >> 4)); else __builtin_amdgcn_s_sleep(2);
            a = ld16(ws, voff, oa); b = ld16(ws, voff, ob); c = ld16(ws, voff, oc); d = ld16(ws, voff, od);
            VMCLR();
            RCADD(1);
            if (t == 32 || !__any(mx(d, mx(c, mx(b, mx(a, 0u)))) == POISON)) break;
        }
    }
}
__device__ __forceinline__ void poll128(GAS u64* f, u64 tag, int lane) { poll(f + lane, tag); poll(f + 64 + lane, tag); }
__device__ __forceinline__ bool tags2(u32x4 v, u64 tag) { return v[0] == (unsigned)tag && v[2] == (unsigned)tag && v[1] == (unsigned)(tag >> 32) && v[3] == (unsigned)(tag >> 32); }
#define BC8(x) __builtin_bit_cast(bf16x8, x)
#define BAR() asm volatile("s_barrier" ::: "memory")

__device__ __forceinline__ float sum8(float x) {
    x += __builtin_bit_cast(float, __builtin_amdgcn_mov_dpp(__builtin_bit_cast(int, x), 0xB1, 0xF, 0xF, true));
    x += __builtin_bit_cast(float, __builtin_amdgcn_mov_dpp(__builtin_bit_cast(int, x), 0x4E, 0xF, 0xF, true));
    x += __builtin_bit_cast(float, __builtin_amdgcn_mov_dpp(__builtin_bit_cast(int, x), 0x141, 0xF, 0xF, true));
    return x;
}
#define XWAIT(A, B, C, D, NL, NC) asm volatile("s_cmp_lt_u32 %4, 4\n\ts_cbranch_scc1 1f\n\ts_waitcnt vmcnt(" #NL ")\n\ts_branch 2f\n1:\n\ts_waitcnt vmcnt(" #NC ")\n2:" \
                                               : "+v"(A), "+v"(B), "+v"(C), "+v"(D) : "s"(w) : "scc", "memory")
template <class Side>
__device__ __forceinline__ void p1_norm(lptr lds, const char* xs, int lane, int w, int rot, Side side) {
    const int pos = lane & 7, row = 8 * w + (lane >> 3);
    const unsigned xo = row * 1024 + pos * 16;
    u32x4 xr[8];
#pragma unroll
    for (int c = 0; c < 8; ++c) asm volatile("global_load_dwordx4 %0, %1, %2" : "=v"(xr[c]) : "v"(xo), "s"(xs + ((c + rot) & 7) * 128) : "memory");
    side(-1);
    float s1 = 0.f, s2 = 0.f, t1 = 0.f, t2 = 0.f;
    XWAIT(xr[0], xr[1], xr[2], xr[3], 10, 4);
    stat2(xr[0], s1, s2); stat2(xr[1], t1, t2); stat2(xr[2], s1, s2); stat2(xr[3], t1, t2);
    PIN(s1); PIN(s2); PIN(t1); PIN(t2);
    XWAIT(xr[4], xr[5], xr[6], xr[7], 6, 0);
    stat2(xr[4], s1, s2); stat2(xr[5], t1, t2); stat2(xr[6], s1, s2); stat2(xr[7], t1, t2);
    s1 = sum8(s1 + t1); s2 = sum8(s2 + t2);
    const float m = s1 * (1.f / D);
    const float rs = __builtin_amdgcn_rsqf(fmaxf(s2 * (1.f / D) - m * m, 0.f) + 1e-5f), nb = -m * rs;
    const unsigned a = row * 128 + ((pos ^ ((row >> 1) & 7)) << 4);
#pragma unroll
    for (int c = 0; c < 8; ++c) {
        u32x4 y;
#pragma unroll
        for (int i = 0; i < 4; ++i) y[i] = pk(fmaf(bflo(xr[c][i]), rs, nb), fmaf(bfhi(xr[c][i]), rs, nb));
        *(LAS u32x4*)(lds + a + ((c + rot) & 7) * 8192) = y;
        side(c);
    }
}
__device__ __forceinline__ void p1_store(rsrc_t ws, int lane, int u, int e, int tile, int hq, u32x4 v) {
    const int head0 = hq * 4 + (u & 1) * 2;
    if (u < 4) st16(ws, lane * 16, (u < 2 ? WS_Q : WS_K) + ((head0 + e) * 64 + tile) * 1024, v);
    else st16(ws, ((lane >> 4) & 1) * 65536 + (lane >> 5) * 256 + (lane & 15) * 16, WS_V + (head0 * 64 + tile) * 1024 + e * 512, v);
}
template <int CG>
__device__ __forceinline__ void p1_consume(lptr lds, rsrc_t ws, int lane, int t, int start, int rb, int hq) {
    const int r = lane & 31, h = lane >> 5;
    const int row = 32 * t + r;
    const unsigned xa = row * 128 + ((h ^ ((row >> 1) & 7)) << 4);
    const int q = (lane & 15) >> 2, p = lane & 3, blk = (lane >> 4) & 1;
    unsigned wa[2];
#pragma unroll
    for (int i = 0; i < 2; ++i) wa[i] = P1_W + h * 3072 + q * 128 + (((i * 4 + blk * 2 + (p >> 1)) ^ (((q >> 1) & 1) << 2)) << 4) + (p & 1) * 8;
    f32x16 acc[3] = {};
    bf16x8 xf[4], wf[4][3];
    auto rdx = [&](int slot, unsigned a) { xf[slot] = lds128(lds + a); };
    auto rdw = [&](int slot, int i, lptr p0, lptr p1, int ks) {
        const int u = CG * 3 + i;
        lptr a = ((u & 1) ? p1 : p0) + (u >> 1) * 1024 + ks * 6144;
        wf[slot][i] = cat(ldstr(a), ldstr(a + 512));
    };
    unsigned xc = (start & 7) * 8192;
    lptr c0 = lds + opaque(wa[0]), c1 = lds + opaque(wa[1]);
    LBAR();
#pragma unroll
    for (int ks = 0; ks < 2; ++ks) { rdx(ks, opaque(xc + (xa ^ (ks << 5)))); rdw(ks, 0, c0, c1, ks); rdw(ks, 1, c0, c1, ks); rdw(ks, 2, c0, c1, ks); }
    SBAR();
#pragma unroll 1
    for (int ch = 0; ch < 8; ++ch) {
        const unsigned xn = ((ch + 1 + start) & 7) * 8192, sn = ((ch + 1) & 3) * P1_SLOT;
        lptr n0 = lds + opaque(sn + wa[0]), n1 = lds + opaque(sn + wa[1]);
        const unsigned xa2 = opaque(xc + (xa ^ 64)), xa3 = opaque(xc + (xa ^ 96)), xb0 = opaque(xn + xa), xb1 = opaque(xn + (xa ^ 32));
#pragma unroll
        for (int ks = 0; ks < 4; ++ks) {
            const int s = (ks + 2) & 3;
            if (ks == 2 && ch < 7) LBAR();
#pragma unroll
            for (int i = 0; i < 3; ++i) {
                if (i == 0) rdx(s, ks == 0 ? xa2 : (ks == 1 ? xa3 : (ks == 2 ? xb0 : xb1)));
                if (ks < 2) rdw(s, i, c0, c1, ks + 2); else rdw(s, i, n0, n1, ks - 2);
                SBAR();
                acc[i] = (CG * 3 + i >= 4) ? MFMA(xf[ks], wf[ks][i], acc[i]) : MFMA(wf[ks][i], xf[ks], acc[i]);
                SBAR();
            }
        }
        xc = xn; c0 = n0; c1 = n1;
    }
#pragma unroll
    for (int i = 0; i < 3; ++i) {
        const float sc = (CG * 3 + i < 2) ? 0.25f * 1.4426950408889634f : 1.f;
#pragma unroll
        for (int e = 0; e < 2; ++e) {
            u32x4 v;
#pragma unroll
            for (int j = 0; j < 4; ++j) v[j] = pk(acc[i][8 * e + 2 * j] * sc, acc[i][8 * e + 2 * j + 1] * sc);
            p1_store(ws, lane, CG * 3 + i, e, rb * 2 + t, hq, v);
        }
    }
}

#define A_EXP(d, s) asm volatile("v_exp_f32 %0, %1" : "=v"(d) : "v"(s))
#define A_CVT(d, a, b) asm volatile("v_cvt_pk_bf16_f32 %0, %1, %2" : "=v"(d) : "v"(a), "v"(b))
#define A_QK(d, k, q) asm volatile("v_mfma_f32_32x32x16_bf16 %0, %1, %2, 0" : "=&v"(d) : "v"(k), "v"(q))
#define A_PV(o, v, p) asm volatile("v_mfma_f32_32x32x16_bf16 %0, %1, %2, %0" : "+v"(o) : "v"(v), "v"(p))
#define UNIT(SC, SN, KN, QN, OPREV, VPREV1, OC, VC0, LOADS) do { \
        float e0, e1, e2, e3, e4, e5, e6, e7; u32x4 p0; \
        A_QK(SN, KN, QN); \
        A_EXP(e0, SC[0]); A_EXP(e1, SC[1]); \
        A_PV(OPREV, VPREV1, p1); \
        LOADS; SBAR(); \
        A_EXP(e2, SC[2]); A_EXP(e3, SC[3]); A_CVT(p0[0], e0, e1); \
        A_EXP(e4, SC[4]); A_EXP(e5, SC[5]); A_CVT(p0[1], e2, e3); \
        A_EXP(e6, SC[6]); A_EXP(e7, SC[7]); A_CVT(p0[2], e4, e5); \
        A_EXP(e0, SC[8]); A_CVT(p0[3], e6, e7); A_EXP(e1, SC[9]); \
        A_EXP(e2, SC[10]); A_PV(OC, VC0, p0); A_EXP(e3, SC[11]); A_CVT(p1[0], e0, e1); \
        A_EXP(e4, SC[12]); A_EXP(e5, SC[13]); A_CVT(p1[1], e2, e3); \
        A_EXP(e6, SC[14]); A_EXP(e7, SC[15]); A_CVT(p1[2], e4, e5); \
        asm volatile("s_nop 0"); A_CVT(p1[3], e6, e7); \
    } while (0)

template <class Hook, class Gap>
__device__ __forceinline__ void attn_main(lptr lds, unsigned ka, unsigned va, unsigned vstep, bf16x8 q0, bf16x8 q1, f32x16& o0, f32x16& o1, Hook hook, Gap gap) {
    f32x16 sa, sb;
    u32x4 p1 = {0u, 0u, 0u, 0u};
    bf16x8 kc, kn, va0, va1, vb0, vb1;
    kc = lds128(lds + ka); va0 = lds128(lds + va); va1 = lds128(lds + va + 512);
    vb1 = va1;
    A_QK(sa, kc, q0);
    asm volatile("s_nop 7\n\ts_nop 7" : "+v"(sa), "+v"(p1), "+v"(o0), "+v"(o1));
    auto settle_a = [&]() { asm volatile("s_nop 3" : "+v"(o0), "+v"(o1), "+v"(sa)); };
    auto settle_b = [&]() { asm volatile("s_nop 3" : "+v"(o0), "+v"(o1), "+v"(sb)); };
#pragma unroll
    for (int c = 0; c < 8; ++c) {
        __builtin_amdgcn_s_setprio(3);
        gap(c, 0, settle_a);
        UNIT(sa, sb, kc, q1, o1, vb1, o0, va0, (kn = lds128(lds + ka + 1024), hook(c, 0)));
        gap(c, 1, settle_b);
        UNIT(sb, sa, kn, q0, o0, va1, o1, va0, (vb0 = lds128(lds + va + 1024), vb1 = lds128(lds + va + 1536), hook(c, 1)));
        __builtin_amdgcn_s_setprio(2);
        gap(c, 2, settle_a);
        UNIT(sa, sb, kn, q1, o1, va1, o0, vb0, (kc = lds128(lds + ka + 2048), hook(c, 2)));
        gap(c, 3, settle_b);
        UNIT(sb, sa, kc, q0, o0, vb1, o1, vb0, (va0 = lds128(lds + va + 2048), va1 = lds128(lds + va + 2560), hook(c, 3)));
        __builtin_amdgcn_s_setprio(1);
        gap(c, 4, settle_a);
        UNIT(sa, sb, kc, q1, o1, vb1, o0, va0, (kn = lds128(lds + ka + 3072), hook(c, 4)));
        gap(c, 5, settle_b);
        UNIT(sb, sa, kn, q0, o0, va1, o1, va0, (vb0 = lds128(lds + va + 3072), vb1 = lds128(lds + va + 3584), hook(c, 5)));
        __builtin_amdgcn_s_setprio(0);
        if (c < 7) { ka += 4096; va += vstep; }
        gap(c, 6, settle_a);
        UNIT(sa, sb, kn, q1, o1, va1, o0, vb0, (kc = lds128(lds + ka), hook(c, 6)));
        gap(c, 7, settle_b);
        UNIT(sb, sa, kc, q0, o0, vb1, o1, vb0, (va0 = lds128(lds + va), va1 = lds128(lds + va + 512), hook(c, 7)));
    }
    asm volatile("s_nop 1\n\tv_mfma_f32_32x32x16_bf16 %0, %2, %3, %0\n\ts_nop 7\n\ts_nop 7" : "+v"(o1), "+v"(sa) : "v"(vb1), "v"(p1));
    asm volatile("" : "+v"(o0));
}

__device__ __forceinline__ void pexp(const f32x16& s, float m, bf16x8& p0, bf16x8& p1) {
    float e[16];
#pragma unroll
    for (int i = 0; i < 16; ++i) e[i] = __builtin_amdgcn_exp2f(s[i] - m);
    p0 = __builtin_bit_cast(bf16x8, (u32x4){pk(e[0], e[1]), pk(e[2], e[3]), pk(e[4], e[5]), pk(e[6], e[7])});
    p1 = __builtin_bit_cast(bf16x8, (u32x4){pk(e[8], e[9]), pk(e[10], e[11]), pk(e[12], e[13]), pk(e[14], e[15])});
}

__global__ __launch_bounds__(512, 2) void fused(const char* __restrict__ x, const char* __restrict__ wqkv, const char* __restrict__ wo,
                                                const char* __restrict__ wg, const char* __restrict__ wu, const char* __restrict__ wd,
                                                char* __restrict__ out, char* ws_) {
    extern __shared__ __attribute__((aligned(16))) char smem[];
    lptr lds = (lptr)smem;
    const unsigned lb = (unsigned)(size_t)lds;
    STAMP_DECL; STAMP(0);
    const int tid = threadIdx.x, lane = tid & 63, w = rfl(tid >> 6);
    const int b = blockIdx.x, xcd = b & 7, li = b >> 3;
    const int r = lane & 31, h = lane >> 5;
    const unsigned l16 = lane * 16;
    GAS u64* fl = (GAS u64*)(ws_ + WS_FL);
    const rsrc_t ws = __builtin_amdgcn_make_buffer_rsrc(ws_, 0, WS_END, 0x00020000);
    const int rb1 = (xcd >> 1) * 8 + (li >> 2), hq = (xcd & 1) * 4 + (li & 3);
    const int head = xcd * 4 + (li & 3), qb = li >> 2;
    const int rb = xcd * 4 + (li >> 3), cb = li & 7;
    const int g4 = (xcd >> 2) * 8 + (li >> 2), j4 = (xcd & 3) * 4 + (li & 3);
    const int start1 = ((li >> 2) + (li & 3) * 2 + xcd) & 7;
    const unsigned woff = (8 * (w & 3) + (lane >> 3)) * 3072 + (((lane & 7) ^ ((lane >> 2) & 4)) << 4);
    const char* wsrc = wqkv + hq * 128;
    auto issue = [&](int s) {
        glds3(wsrc + (size_t)((s + 2 * start1) & 15) * 32 * 3072, woff, lb + P1_W + (s & 7) * 12288 + (w & 3) * 3072);
    };
    unsigned tlbw = 0;
    auto issue_l = [&](int s) {
        glds3_upper(w, wsrc + (size_t)((s + 2 * start1) & 15) * 32 * 3072, woff, lb + P1_W + (s & 7) * 12288 + (w & 3) * 3072);
    };
    p1_norm(lds, x + (size_t)rb1 * 65536, lane, w, (li & 3) * 2, [&](int c) __attribute__((always_inline)) {
        if (c < 0) {
            issue_l(0); issue_l(1);
            const int ix = li * 8 + w;
            const char* pc; int cl0, cl1;
            asm volatile("s_getpc_b64 %0\n.Lpf_here:\n\ts_mov_b32 %1, .Lpf_end0-.Lpf_here\n\ts_mov_b32 %2, .Lpf_end1-.Lpf_here" : "=s"(pc), "=s"(cl0), "=s"(cl1));
            const int clen = cl0 > cl1 ? cl0 : cl1;
#pragma unroll
            for (int k = 0; k < 6; ++k) touch(tlbw, pc + (ix + 256 * k) * 64, (ix + 256 * k) * 64 + 4, 0, clen);
            touch(tlbw, ws_ + (size_t)ix * 262144, ix, 0, 96);
            touch(tlbw, wg + (size_t)(ix - 96) * 65536, ix, 96, 28); touch(tlbw, wu + (size_t)(ix - 124) * 65536, ix, 124, 28);
            touch(tlbw, wd + (size_t)(ix - 152) * 65536, ix, 152, 28); touch(tlbw, wo + (size_t)(ix - 180) * 65536, ix, 180, 8);
            touch(tlbw, out + (size_t)(ix - 188) * 65536, ix, 188, 32);
        } else if (c == 2) issue_l(2); else if (c == 6) issue_l(3);
    });
    asm volatile("s_waitcnt lgkmcnt(0)" : "+s"(tlbw) :: "memory");
    STAMP(7); STAMPM(1, 1);
    u64 e_raw = __hip_atomic_load(fl + FL_E, RLX);
    asm volatile("" :: "s"(wo), "s"(wg), "s"(wu), "s"(wd), "s"(out));
    const int qp = w & 3, kh = w >> 2;
    const unsigned ka0 = S0 + kh * 32768 + l16;
    unsigned va0, vstep;
    if (r < 16) { va0 = S2 + kh * 32768 + h * 256 + r * 16; vstep = 4096; }
    else { va0 = M_CONST + ((r >= 20 && r < 28) ? (r == 20 ? 64 : 80) : (r == 16 ? 0 : 16)); vstep = 0; }

    const u32x4 poison = {POISON, POISON, POISON, POISON};
    const char* ksrc = ws_ + WS_K + (size_t)head * 65536;

    GAS u64* myf2 = fl + FL_2 + (qb * 8 + qp * 2 + kh) * 32 + head;
    auto attn_finish = [&](f32x16& o0, f32x16& o1, bf16x8 q0, bf16x8 q1, u64 tag) __attribute__((always_inline)) {
    STAMP(5); STAMPM(4, 15);
    const int lane = fresh_lane();
    {
        LAS float* xo = (LAS float*)(lds + M_X + w * 2304);
        LAS float* xi = (LAS float*)(lds + M_X + (w ^ 4) * 2304);
        const unsigned odst = WS_O + ((qb * 8 + qp * 2 + kh) * 32 + head) * 1024;
        float mag = 0.f;
#pragma unroll
        for (int i = 0; i < 8; ++i) mag = fmaf(fabsf(o0[i]), 0.0625f, fmaf(fabsf(o1[i]), 0.0625f, mag));
        const bool ok = (mag <= 5.0e36f) & (o0[8] >= 0x1p-31f) & (o0[8] <= 0x1p99f) & (o1[8] >= 0x1p-31f) & (o1[8] <= 0x1p99f);
        if (!ok) *(LAS unsigned*)(lds + M_WORD) = 1u;
#pragma unroll
        for (int i = 0; i < 9; ++i) xo[i * 64 + lane] = kh ? o0[i] : o1[i];
        VMCLR();
        STAMPM(2, 1);
        LBAR();
        STAMPM(2, 2);
        float o[9];
#pragma unroll
        for (int i = 0; i < 9; ++i) o[i] = (kh ? o1[i] : o0[i]) + xi[i * 64 + lane];
        if (rfl(*(LAS unsigned*)(lds + M_WORD))) {
            LBAR();
            const f32x16 zero = {};
            float m0 = -INFINITY, m1 = -INFINITY;
            unsigned ka = ka0, va = va0;
#pragma unroll 1
            for (int i = 0; i < 32; ++i) {
                const bf16x8 kf = lds128(lds + ka);
                const f32x16 s0 = MFMA(kf, q0, zero), s1 = MFMA(kf, q1, zero);
#pragma unroll
                for (int j = 0; j < 16; ++j) { m0 = fmaxf(m0, s0[j]); m1 = fmaxf(m1, s1[j]); }
                ka += 1024;
            }
            m0 = fmaxf(m0, __shfl_xor(m0, 32)); m1 = fmaxf(m1, __shfl_xor(m1, 32));
            xo[lane] = m0; xo[64 + lane] = m1;
            LBAR();
            m0 = fmaxf(m0, xi[lane]); m1 = fmaxf(m1, xi[64 + lane]);
            LBAR();
            o0 = zero; o1 = zero; ka = ka0;
#pragma unroll 1
            for (int i = 0; i < 32; ++i) {
                const unsigned vo = va + (i & 3) * 1024;
                const bf16x8 kf = lds128(lds + ka), v0 = lds128(lds + vo), v1 = lds128(lds + vo + 512);
                bf16x8 pa, pb;
                pexp(MFMA(kf, q0, zero), m0, pa, pb); o0 = MFMA(v0, pa, o0); o0 = MFMA(v1, pb, o0);
                SBAR();
                pexp(MFMA(kf, q1, zero), m1, pa, pb); o1 = MFMA(v0, pa, o1); o1 = MFMA(v1, pb, o1);
                ka += 1024; if ((i & 3) == 3) va += vstep;
            }
#pragma unroll
            for (int i = 0; i < 9; ++i) xo[i * 64 + lane] = kh ? o0[i] : o1[i];
            LBAR();
#pragma unroll
            for (int i = 0; i < 9; ++i) o[i] = (kh ? o1[i] : o0[i]) + xi[i * 64 + lane];
        }
        const float inv = __builtin_amdgcn_rcpf(o[8]);
        st16(ws, l16, odst, (u32x4){pk(o[0] * inv, o[1] * inv), pk(o[2] * inv, o[3] * inv), pk(o[4] * inv, o[5] * inv), pk(o[6] * inv, o[7] * inv)});
    }
    if (b == 0 && w == 0) publish(fl + FL_E, tag, lane);
    STAMP(6); STAMPM(2, 3);
    };
    if (w >= 4) {
        f32x16 o0 = {}, o1 = {};
        bf16x8 q0, q1;
        u32x4 stash[32];
        u64 tag;
        f32x4 csA, csB;
        const int l = w - 4;
        u32x4 jr[2][4];
        WAITBAR(7); STAMPM(1, 2); issue(4); issue(5); issue(6); issue(7); STAMPM(1, 3);
        WAITBAR(12); STAMPM(1, 4); issue(8); issue(9); STAMPM(1, 5);
        WAITBAR(12); STAMPM(1, 6); issue(10); issue(11);
        WAITBAR(12); STAMPM(1, 7); issue(12); issue(13);
        WAITBAR(12); STAMPM(1, 8); issue(14); issue(15); STAMPM(1, 9);
        int bj = b;
        asm volatile("" : "+s"(bj));
        const int krow = lane >> 3, ch = lane & 7;
        const bool isg = bj < 112, isd = bj < 224;
        const int j0 = (isd ? bj - 112 : bj - 224) * 8 + 2 * l;
        const int nks = (isd && !isg) ? 112 : 32;
        const int lc = isg ? bj >> 2 : (isd ? j0 / 112 : j0 >> 5), ks0 = isg ? (bj & 3) * 8 + 2 * l : j0 - lc * nks;
        const unsigned rstride = isg ? 3584 : 1024;
        const unsigned jo = (ks0 * 16 + krow) * rstride + lc * 128 + ch * 16;
        const char* jsrc = isg ? wg : (isd ? wd : wo);
        const unsigned jbase = isg ? WS_WGU + lc * 4 * 32768 : (isd ? WS_WD : WS_WO) + lc * 2 * nks * 1024;
        const bool perm = isd && !isg;
        const unsigned jdst0 = jbase + (perm ? (ks0 % 7) * 16 + ks0 / 7 : ks0) * 1024, jdst1 = jbase + (perm ? ((ks0 + 1) % 7) * 16 + (ks0 + 1) / 7 : ks0 + 1) * 1024;
        const unsigned jvo = isg ? l16 : WS_END;
        {
            const rsrc_t ra = __builtin_amdgcn_make_buffer_rsrc((void*)jsrc, 0, isd ? 1835008 : 524288, 0x00020000);
            const rsrc_t rb2 = __builtin_amdgcn_make_buffer_rsrc((void*)wu, 0, isg ? 1835008 : 0, 0x00020000);
#pragma unroll
            for (int j = 0; j < 2; ++j) {
                jr[j][0] = __builtin_amdgcn_raw_buffer_load_b128(ra, jo + j * 16 * rstride, 0, 2); jr[j][1] = __builtin_amdgcn_raw_buffer_load_b128(ra, jo + (j * 16 + 8) * rstride, 0, 2);
                jr[j][2] = __builtin_amdgcn_raw_buffer_load_b128(rb2, jo + j * 16 * rstride, 0, 2); jr[j][3] = __builtin_amdgcn_raw_buffer_load_b128(rb2, jo + (j * 16 + 8) * rstride, 0, 2);
            }
        }
        STAMPM(1, 10); STAMPM(5, 1);
        WAITBAR(20); STAMPM(1, 11); WAITBAR(14); STAMPM(1, 12); WAITBAR(8); STAMPM(1, 13);
            LBAR();
        STAMP(1); STAMPM(1, 14); STAMPM(5, 2);
        VMCLR();
        STAMPM(5, 3);
        PIN(e_raw);
        tag = ((u64)(unsigned)rfl((int)(e_raw >> 32)) << 32 | (unsigned)rfl((int)e_raw)) + 1;
        if (l == 1) {
            const unsigned v = ((lane & 3) == 0) ? ONES2 : 0u;
            *(LAS u32x4*)(lds + M_CONST + (lane >> 3) * 512 + (lane & 7) * 16) = (u32x4){v, v, v, v};
            if (lane == 0) *(LAS unsigned*)(lds + M_WORD) = 0u;
        }
        u32x4 tf[8];
        f32x16 csa = {};
        {
            lptr st = lds + M_STG + l * 2048;
            lptr ta = st + (4 * h + ((lane & 15) >> 2)) * 64 + ((lane >> 4) & 1) * 32 + (lane & 3) * 8;
            if (isg) {
                lptr sw = st + krow * 64 + ((ch >> 1) & 1) * 1024 + (ch & 1) * 16;
#pragma unroll
                for (int i = 0; i < 4; ++i) {
                    const int j = i >> 1, rr = i & 1;
                    if ((ch >> 2) == rr) {
                        *(LAS u32x4*)sw = jr[j][0]; *(LAS u32x4*)(sw + 512) = jr[j][1];
                        *(LAS u32x4*)(sw + 32) = jr[j][2]; *(LAS u32x4*)(sw + 512 + 32) = jr[j][3];
                    }
#pragma unroll
                    for (int k = 0; k < 2; ++k) {
                        const bf16x8 f = cat(ldstr(ta + k * 1024), ldstr(ta + k * 1024 + 512));
                        const unsigned sel = (r == 2 * rr + k) ? ONES2 : 0u;
                        csa = MFMA(BC8(((u32x4){sel, sel, sel, sel})), f, csa);
                        tf[j * 4 + 2 * rr + k] = __builtin_bit_cast(u32x4, f);
                    }
                }
            } else {
                lptr sw = st + krow * 64 + (ch >> 2) * 1024 + (ch & 3) * 16;
#pragma unroll
                for (int j = 0; j < 2; ++j) {
                    *(LAS u32x4*)sw = jr[j][0]; *(LAS u32x4*)(sw + 512) = jr[j][1];
#pragma unroll
                    for (int k = 0; k < 2; ++k) {
                        tf[j * 4 + k] = __builtin_bit_cast(u32x4, cat(ldstr(ta + k * 1024), ldstr(ta + k * 1024 + 512)));
                        tf[j * 4 + 2 + k] = tf[j * 4 + k];
                    }
                }
            }
        }
        const u32x4 csv = {__float_as_uint(csa[0]), __float_as_uint(csa[1]), __float_as_uint(csa[2]), __float_as_uint(csa[3])};
        if (l == 0) {
            STAMPM(5, 4);
            __builtin_amdgcn_s_sleep(SL1);
            STAMPM(5, 5);
            poll(fl + FL_1 + (head >> 2) * 128 + qb * 16, 16, tag, lane);
            STAMP(4); STAMPM(5, 6);
            gld8(ksrc - (WS_K - WS_Q) + qb * 8192, l16, lb + M_Q);
            gld4(ksrc + qb * 8192, l16, lb + S0); gld4(ksrc + qb * 8192 + 4096, l16, lb + S1);
            gld4(ksrc + (WS_V - WS_K) + qb * 8192, l16, lb + S2); gld4(ksrc + (WS_V - WS_K) + qb * 8192 + 4096, l16, lb + S3);
        }
        u32x4 cp[8];
        u64 fa, fc;
        STAMPM(5, 7);
        WAITBAR(0);
        STAMP(3); STAMPM(5, 8);
        q0 = lds128(lds + M_Q + qp * 2048 + l16); q1 = lds128(lds + M_Q + qp * 2048 + 1024 + l16);
        const unsigned sdo = WS_WD + (cb * 2 * 112 + 4 * l) * 1024;
        GAS u64* fra = fl + FL_R + 4 * (112 + 14 * cb) + (lane < 56 ? lane : 55);
        GAS u64* frc = fl + FL_R + 16 * ((7 * j4) >> 2) + (lane < 16 * (((7 * j4 + 6) >> 2) - ((7 * j4) >> 2) + 1) ? lane : 0);
        attn_main(lds, ka0, va0, vstep, q0, q1, o0, o1, [&](int c, int i) __attribute__((always_inline)) {
            const int n = c * 8 + i;
            if (n >= 1 && n < 17 && (n & 1) == 1) {
                const int m = n >> 1, k = m & 3;
                st16(ws, k < 2 ? l16 : jvo, ((m >> 2) ? jdst1 : jdst0) + k * nks * 1024, tf[m]);
            }
            if (n == 16) st16(ws, (isg && h == 0) ? r * 16 : WS_END, WS_CS + (((b & 3) * 4 + l) * 28 + lc) * 512, csv);
            if (n == 32) { fa = __hip_atomic_load(fra, RLX); fc = __hip_atomic_load(frc, RLX); }
            if (n >= 38 && n < 54) {
                const int m = n - 38;
                stash[2 * m] = ld16(ws, l16, sdo + ((m >> 2) * 16 + (m & 3)) * 1024);
                stash[2 * m + 1] = ld16(ws, l16, sdo + (112 + (m >> 2) * 16 + (m & 3)) * 1024);
            }
            if (n == 35) {
#pragma unroll
                for (int k = 0; k < 4; ++k) {
                    const unsigned so = WS_CS + ((4 * l + k) * 28 + ((7 * j4) >> 2)) * 512;
                    cp[k] = ld16(ws, l16, so); cp[4 + k] = ld16(ws, r * 16, so + 1024);
                }
            }
        }, [&](int c, int i, auto settle) __attribute__((always_inline)) {
            const int n = c * 8 + i;
            if (n == 2) STAMPM(4, 1); if (n == 6) STAMPM(4, 2); if (n == 7) STAMPM(4, 3); if (n == 14) STAMPM(4, 4); if (n == 15) STAMPM(4, 5); if (n == 20) STAMPM(4, 6); if (n == 21) STAMPM(4, 7);
            if (n == 24) STAMPM(4, 8); if (n == 32) STAMPM(4, 9); if (n == 36) STAMPM(4, 10); if (n == 40) STAMPM(4, 11); if (n == 48) STAMPM(4, 12); if (n == 56) STAMPM(4, 13); if (n == 60) STAMPM(4, 14);
            if (n == 6 || n == 14) {
                BAR();
            } else if (n == 20) {
                settle();
                VMWAIT(0);
                publish(fl + FL_R + b * 4 + l, tag, lane);
            } else if (n == 35) {
                settle();
                PIN(fa); PIN(fc);
                if (!__all(fa == tag && fc == tag)) { RCINC(6, 1); poll(fra, tag); poll(frc, tag); }
            } else if (n == 38) {
                asm volatile("" : "+v"(cp[0]), "+v"(cp[1]), "+v"(cp[2]), "+v"(cp[3]), "+v"(cp[4]), "+v"(cp[5]), "+v"(cp[6]), "+v"(cp[7]));
                csA = (__builtin_bit_cast(f32x4, cp[0]) + __builtin_bit_cast(f32x4, cp[1])) + (__builtin_bit_cast(f32x4, cp[2]) + __builtin_bit_cast(f32x4, cp[3]));
                csB = (__builtin_bit_cast(f32x4, cp[4]) + __builtin_bit_cast(f32x4, cp[5])) + (__builtin_bit_cast(f32x4, cp[6]) + __builtin_bit_cast(f32x4, cp[7]));
                PIN(csA); PIN(csB);
            }
        });
        attn_finish(o0, o1, q0, q1, tag);
        const char* gu = ws_ + WS_WGU + (size_t)j4 * 7 * 32768 + l * 8192;
        const char* wdn = ws_ + WS_WD + (size_t)((cb * 2 + (l >> 1)) * 112 + 80) * 1024 + (l & 1) * 8192;
        const unsigned lq = lb + l * 8192;
        {
            gld8(gu, l16, lq + S0);
            STAMPM(2, 4);
            __builtin_amdgcn_s_sleep(SL2);
            STAMP(7);
            const char* os = ws_ + WS_O + (size_t)(rb * 2 + (l >> 1)) * 32768 + (l & 1) * 4096;
            const unsigned lo = lb + S3 + l * 4096;
            gld4(os, l16, lo); gld4(os + 8192, l16, lo + 16384);
            STAMPM(2, 5);
            WAITBAR(4);
            STAMPM(2, 6);
            gld4(os + 16384, l16, lo + 32768); gld4(os + 24576, l16, lo + 49152);
            publish(myf2, tag, lane);
            STAMPM(2, 7);
            WAITBAR(9); STAMPM(2, 8); gld8(gu + 32768, l16, lq + S1); STAMPM(2, 9);
            WAITBAR(13);
            WAITBAR(9);
        }
        LBAR();
        STAMPM(2, 10);
        gld8(gu + 2 * 32768, l16, lq + S2); gld8(gu + 3 * 32768, l16, lq + S3);
        STAMPM(2, 11);
        if (l == 0 && lane == 0) *(LAS unsigned*)(lds + M_CNT) = 0u;
        *(LAS f32x4*)(lds + M0 + 1024 + 2048 * l + l16) = csA; *(LAS f32x4*)(lds + M0 + 1024 + 2048 * l + 1024 + l16) = csB;
        STAMPM(2, 12);
        WAITBAR(8);
        {
            const int ft = ((7 * j4) & 3) + 2 * l + h;
            LAS float* p = (LAS float*)(lds + M0 + 1024 + (ft >> 2) * 512 + r * 16 + (ft & 3) * 4);
            *(LAS float*)(lds + M0 + (l * 64 + lane) * 4) = (p[0] + p[512]) + (p[1024] + p[1536]);
        }
        WAITBAR(0);
        STAMPM(3, 1);
        gld8(gu + 4 * 32768, l16, lq + S0); gld8(gu + 5 * 32768, l16, lq + S1); gld8(gu + 6 * 32768, l16, lq + S2);
        STAMPM(3, 2);
        WAITBAR(16); STAMPM(3, 3); gld8(wdn, l16, lq + S3); STAMPM(3, 4);
        WAITBAR(16); STAMPM(3, 5); gld8(wdn + 16384, l16, lq + S0); STAMPM(3, 6);
        WAITBAR(0); STAMPM(3, 7);
        f32x16 C[4];
        u32x4 af[20][2], wx[8];
        constexpr int AH = 6;
        auto aoff = [&](int k, int t) { return WS_ACT + ((rb * 2 + t) * 112 + (4 * l + (k & 3)) * 7 + (k >> 2)) * 1024; };
#pragma unroll
        for (int k = 0; k < AH; ++k) { af[k][0] = ld16(ws, l16, aoff(k, 0)); af[k][1] = ld16(ws, l16, aoff(k, 1)); }
        SBAR();
#pragma unroll
        for (int k = 0; k < 20; ++k) {
            if (k == 0) STAMPM(3, 9);
            if ((k & 1) == 0) verify4(af[k][0], af[k][1], af[k + 1][0], af[k + 1][1], ws, l16, aoff(k, 0), aoff(k, 1), aoff(k + 1, 0), aoff(k + 1, 1), fl + FL_4 + rb * 32, 32, tag RC(5));
            if (k == 0) STAMPM(3, 10); if (k == 4) STAMPM(3, 11); if (k == 8) STAMPM(3, 12); if (k == 16) STAMPM(3, 13);
#pragma unroll
            for (int q = 0; q < 4; ++q) C[q] = MFMA(BC8(k < 16 ? stash[2 * k + (q >> 1)] : wx[2 * (k - 16) + (q >> 1)]), BC8(af[k][q & 1]), k ? C[q] : f32x16{});
            SBAR();
            if (k + AH < 20) { af[k + AH][0] = ld16(ws, l16, aoff(k + AH, 0)); af[k + AH][1] = ld16(ws, l16, aoff(k + AH, 1)); }
            if (k >= 6 && k < 10) {
                const unsigned wo4 = WS_WD + (cb * 2 * 112 + 64 + 4 * l + k - 6) * 1024;
                wx[2 * (k - 6)] = ld16(ws, l16, wo4); wx[2 * (k - 6) + 1] = ld16(ws, l16, wo4 + 112 * 1024);
            }
            SBAR();
        }
        STAMP(12); STAMPM(3, 14);
        if (l >= 2) {
#pragma nounroll
            for (int t = 0; t < (1 << 20); ++t) {
                if (rfl(__hip_atomic_load((LAS unsigned*)(lds + M_CNT), __ATOMIC_RELAXED, __HIP_MEMORY_SCOPE_WORKGROUP)) == 4) break;
                __builtin_amdgcn_s_sleep(1);
            }
            asm volatile("" ::: "memory");
        }
        lptr pp = lds + opaque(S1 + l * 16384 + l16);
#pragma unroll
        for (int q = 0; q < 4; ++q)
#pragma unroll
            for (int g = 0; g < 4; ++g) *(LAS f32x4*)(pp + q * 4096 + g * 1024) = (f32x4){C[q][4 * g], C[q][4 * g + 1], C[q][4 * g + 2], C[q][4 * g + 3]};
        STAMP(14);
        LBAR();
        asm volatile(".Lpf_end0:");
    } else {
        f32x16 o0 = {}, o1 = {};
        bf16x8 q0, q1;
        u32x4 stash[32];
        u64 tag;
        if (w >> 1) p1_consume<1>(lds, ws, lane, w & 1, start1, rb1, hq); else p1_consume<0>(lds, ws, lane, w & 1, start1, rb1, hq);
        STAMP(1); STAMPM(1, 14); STAMPM(5, 2);
        LBAR();
        VMWAIT(0);
        PIN(e_raw);
        tag = ((u64)(unsigned)rfl((int)(e_raw >> 32)) << 32 | (unsigned)rfl((int)e_raw)) + 1;
        publish(fl + FL_1 + (hq * 32 + rb1) * 4 + w, tag, lane);
        STAMP(2); STAMPM(5, 3);
        LBAR();
        STAMP(3);
        q0 = lds128(lds + M_Q + qp * 2048 + l16); q1 = lds128(lds + M_Q + qp * 2048 + 1024 + l16);
        const char* dsrc = ksrc + (w >> 1) * (WS_V - WS_K) + (w & 1) * 4096;
        const unsigned ddst = lb + w * 32768;
        const unsigned swo = WS_WO + (cb * 2 + (w >> 1)) * 32768;
        GAS u64* frb = fl + FL_R + 4 * (224 + 4 * cb) + (lane & 15);
        u32x4 f1, fp;
        bool fpok;
        u64 fb;
        attn_main(lds, ka0, va0, vstep, q0, q1, o0, o1, [&](int c, int i) __attribute__((always_inline)) {
            const int n = c * 8 + i;
            if (n == 0) f1 = ld16(ws, l16, WS_FL + (FL_1 + (head >> 2) * 128) * 8);
            if (n >= 2 && n < 9) gld4(dsrc + ((qb + n - 1) & 7) * 8192, l16, ddst + (n - 1) * 4096);
            if (n >= 14 && n < 38 && (n & 1) == 0) {
                const int m = (n - 14) >> 1;
                if (m < 2) st16(ws, l16, WS_O + ((qb * 8 + 2 * w + m) * 32 + head) * 1024, poison);
                else if (m < 4) st16(ws, l16, WS_H + ((rb * 2 + (w & 1)) * 32 + cb * 4 + (w >> 1) * 2 + m - 2) * 1024, poison);
                else if (m == 4) st16(ws, r * 16, WS_HS + ((rb * 2 + (w & 1)) * 8 + cb) * 512, poison);
                else st16(ws, l16, WS_ACT + ((g4 * 4 + w) * 112 + j4 * 7 + m - 5) * 1024, poison);
            }
            if (n == 37) fb = __hip_atomic_load(frb, RLX);
            if (n >= 41 && n < 55) {
                stash[2 * (n - 41)] = ld16(ws, l16, swo + 2 * (n - 41) * 1024);
                stash[2 * (n - 41) + 1] = ld16(ws, l16, swo + (2 * (n - 41) + 1) * 1024);
            }
            if (n == 56 || n == 59) fp = ld16(ws, l16, WS_FL + FL_P * 8 + w * 2048 + (n == 59 ? 1024 : 0));
        }, [&](int c, int i, auto settle) __attribute__((always_inline)) {
            const int n = c * 8 + i;
            if (n == 2) STAMPM(4, 1); if (n == 6) STAMPM(4, 2); if (n == 7) STAMPM(4, 3); if (n == 14) STAMPM(4, 4); if (n == 15) STAMPM(4, 5); if (n == 20) STAMPM(4, 6); if (n == 21) STAMPM(4, 7);
            if (n == 24) STAMPM(4, 8); if (n == 32) STAMPM(4, 9); if (n == 36) STAMPM(4, 10); if (n == 40) STAMPM(4, 11); if (n == 48) STAMPM(4, 12); if (n == 56) STAMPM(4, 13); if (n == 60) STAMPM(4, 14);
            if (n == 2) {
                settle();
                PIN(f1);
                if (!__all(tags2(f1, tag))) { RCINC(0, 1); poll128(fl + FL_1 + (head >> 2) * 128, tag, lane); }
            } else if (n == 6) { VMWAIT(12); BAR(); }
            else if (n == 14) { VMWAIT(0); BAR(); }
            else if (n == 40) {
                settle();
                VMCLR();
                publish(fl + FL_P + b * 4 + w, tag, lane);
                if (!__all(fb == tag)) { RCINC(6, 1); poll(frb, tag); }
            } else if (n == 59) {
                PIN(fp);
                fpok = __all(tags2(fp, tag));
            } else if (n == 62) {
                settle();
                PIN(fp);
                if (!(fpok && __all(tags2(fp, tag)))) { RCINC(7, 1); poll128(fl + FL_P + w * 256, tag, lane); poll128(fl + FL_P + w * 256 + 128, tag, lane); }
            }
        });
        attn_finish(o0, o1, q0, q1, tag);
        {
        const int lane = fresh_lane(), r = lane & 31, h = lane >> 5;
        const int tt = w & 1, ct = w >> 1;
        const int ttile = rb * 2 + tt, ctile = cb * 2 + ct;
        u32x2 rx[4];
        {
            const char* p = x + (size_t)(ttile * 32 + r) * 1024 + (ctile * 32 + 4 * h) * 2;
#pragma unroll
            for (int g = 0; g < 4; ++g) rx[g] = *(const u32x2*)(p + g * 16);
        }
        u32x4 hr[2];
        {
            f32x16 acc = {};
            lptr ba = lds + opaque(S3 + tt * 8192 + l16);
#pragma unroll
            for (int m = 28; m < 32; ++m) stash[m] = ld16(ws, l16, swo + m * 1024);
            VMWAIT(8);
            publish(myf2, tag, lane);
            STAMPM(2, 4);
#pragma unroll
            for (int qd = 0; qd < 4; ++qd) {
                LBAR();
                STAMPM(2, 5 + qd);
                if (qd == 0) STAMP(8);
                u32x4 bf[8];
#pragma unroll
                for (int ks = 0; ks < 8; ++ks) bf[ks] = *(LAS u32x4*)(ba + qd * 16384 + ks * 1024);
                SBAR();
#pragma unroll
                for (int g = 0; g < 2; ++g) {
                    const unsigned oo = WS_O + (ttile * 32 + qd * 8 + g * 4) * 1024;
                    verify4(bf[4 * g], bf[4 * g + 1], bf[4 * g + 2], bf[4 * g + 3], ws, l16, oo, oo + 1024, oo + 2048, oo + 3072, fl + FL_2 + ttile * 32, 32, tag RC(2));
#pragma unroll
                    for (int ks = 4 * g; ks < 4 * g + 4; ++ks) acc = MFMA(BC8(stash[qd * 8 + ks]), BC8(bf[ks]), acc);
                }
            }
#pragma unroll
            for (int g = 0; g < 4; ++g) {
                hr[g >> 1][(g & 1) * 2] = pk(acc[4 * g] + bflo(rx[g][0]), acc[4 * g + 1] + bfhi(rx[g][0]));
                hr[g >> 1][(g & 1) * 2 + 1] = pk(acc[4 * g + 2] + bflo(rx[g][1]), acc[4 * g + 3] + bfhi(rx[g][1]));
            }
            st16(ws, l16, WS_H + (ttile * 32 + ctile * 2) * 1024, hr[0]);
            st16(ws, l16, WS_H + (ttile * 32 + ctile * 2 + 1) * 1024, hr[1]);
            float s1 = 0.f, s2 = 0.f;
            stat2(hr[0], s1, s2); stat2(hr[1], s1, s2);
            s1 = halfsum(s1); s2 = halfsum(s2);
            if (h == 0) __hip_atomic_store((GAS u64*)(ws_ + WS_HS) + ((ttile * 8 + cb) * 32 + r) * 2 + ct, (u64)__float_as_uint(s1) | ((u64)__float_as_uint(s2) << 32), RLX);
        }
        STAMPM(2, 9);
        LBAR();
        STAMP(9); STAMPM(2, 10);
        const int t4 = g4 * 4 + w;
        __builtin_amdgcn_s_sleep(SL3);
        u32x4 hs[4], hf[32];
        constexpr int HAHEAD = 12;
        const unsigned ho = WS_H + t4 * 32768;
#pragma unroll
        for (int i = 0; i < 4; ++i) hs[i] = ld16(ws, l16 + i * 1024, WS_HS + t4 * 4096);
#pragma unroll
        for (int ks = 0; ks < HAHEAD; ++ks) hf[ks] = ld16(ws, l16, ho + ks * 1024);
        STAMPM(2, 11);
        VMWAIT(4 + HAHEAD);
        STAMPM(2, 12);
        publish(fl + FL_3 + ttile * 16 + ctile, tag, lane);
        LBAR();
        STAMPM(2, 13);
        { const unsigned so = WS_HS + t4 * 4096; verify4(hs[0], hs[1], hs[2], hs[3], ws, l16, so, so + 1024, so + 2048, so + 3072, fl + FL_3 + t4 * 16, 16, tag RC(3)); }
        STAMP(10); STAMPM(2, 14);
        float s1 = 0.f, s2 = 0.f;
#pragma unroll
        for (int i = 0; i < 4; ++i) { s1 += __uint_as_float(hs[i][0]) + __uint_as_float(hs[i][2]); s2 += __uint_as_float(hs[i][1]) + __uint_as_float(hs[i][3]); }
        s1 = halfsum(s1); s2 = halfsum(s2);
        const float m = s1 * (1.f / D);
        const float vv = fmaxf(s2 * (1.f / D) - m * m, 0.f) + 1e-5f;
        const float rs = __builtin_amdgcn_rsqf(vv);
        constexpr float C1 = -2.f * 0.7978845608028654f * 1.4426950408889634f, C2 = C1 * 0.044715f;
        const float k1 = C1 * rs, k2 = C2 * rs * rs * rs;
        SBAR();
        f32x16 A[4];
        lptr wlo = lds + opaque(l16), whi = lds + opaque(S2 + l16);
        {
            bf16x8 wf[2][3];
            wf[0][0] = lds128(wlo); wf[0][1] = lds128(wlo + S1); wf[0][2] = lds128(whi);
            SBAR();
#pragma unroll
            for (int ks = 0; ks < 32; ++ks) {
                const int c = ks & 1, n = c ^ 1;
                if ((ks & 3) == 0) verify4(hf[ks], hf[ks + 1], hf[ks + 2], hf[ks + 3], ws, l16, ho + ks * 1024, ho + (ks + 1) * 1024, ho + (ks + 2) * 1024, ho + (ks + 3) * 1024, fl + FL_3 + t4 * 16, 16, tag RC(4));
                if (ks + HAHEAD < 32) hf[ks + HAHEAD] = ld16(ws, l16, ho + (ks + HAHEAD) * 1024);
                if (ks < 31) wf[n][0] = lds128(wlo + (ks + 1) * 1024);
                SBAR(); A[0] = MFMA(wf[c][0], BC8(hf[ks]), ks ? A[0] : f32x16{}); SBAR();
                if (ks < 31) wf[n][1] = lds128(wlo + S1 + (ks + 1) * 1024);
                SBAR(); A[1] = MFMA(wf[c][1], BC8(hf[ks]), ks ? A[1] : f32x16{}); SBAR();
                if (ks < 31) wf[n][2] = lds128(whi + (ks + 1) * 1024);
                SBAR(); A[2] = MFMA(wf[c][2], BC8(hf[ks]), ks ? A[2] : f32x16{}); SBAR();
            }
        }
        LAS float* cs = (LAS float*)(lds + opaque(M0 + 16 * h));
        const unsigned adst = WS_ACT + (t4 * 112 + j4 * 7) * 1024;
        float ea[4], eu[4], et[4];
        f32x4 cg, cu;
        u32x4 eo;
        auto epi = [&](auto G_) __attribute__((always_inline)) {
            constexpr int G = decltype(G_)::v;
            constexpr int hf_ = G / 13, st = G % 13, t = hf_ >> 1, e0 = (hf_ & 1) * 4;

            if constexpr (st == 0) {
#pragma unroll
                for (int e = 0; e < 4; ++e) { ea[e] = fmaf(-m, cg[e], A[t & 3][e0 + e]); PIN(ea[e]); }
            } else if constexpr (st == 1) {
#pragma unroll
                for (int e = 0; e < 4; ++e) { eu[e] = fmaf(-m, cu[e], A[t & 3][8 + e0 + e]); PIN(eu[e]); }
            } else if constexpr (st == 2) {
#pragma unroll
                for (int e = 0; e < 4; ++e) { et[e] = ea[e] * ea[e]; PIN(et[e]); }
            } else if constexpr (st == 3) {
#pragma unroll
                for (int e = 0; e < 4; ++e) { et[e] = fmaf(k2, et[e], k1); PIN(et[e]); }
            } else if constexpr (st == 4) {
#pragma unroll
                for (int e = 0; e < 4; ++e) { et[e] *= ea[e]; PIN(et[e]); }
            } else if constexpr (st == 5) {
#pragma unroll
                for (int e = 0; e < 4; ++e) { eu[e] *= ea[e]; PIN(eu[e]); }
            } else if constexpr (st == 6 || st == 7) {
#pragma unroll
                for (int e = 2 * (st - 6); e < 2 * (st - 6) + 2; ++e) { et[e] = __builtin_amdgcn_exp2f(et[e]); PIN(et[e]); }
                if constexpr (hf_ + 1 < 14) {
                    LAS float* c = cs + ((hf_ + 1) >> 1) * 32 + ((hf_ + 1) & 1) * 8;
                    if (st == 6) cg = *(LAS f32x4*)c; else cu = *(LAS f32x4*)(c + 16);
                }
            } else if constexpr (st == 8) {
#pragma unroll
                for (int e = 0; e < 4; ++e) { et[e] = fmaf(et[e], vv, vv); PIN(et[e]); }
            } else if constexpr (st == 9 || st == 10) {
#pragma unroll
                for (int e = 2 * (st - 9); e < 2 * (st - 9) + 2; ++e) { et[e] = __builtin_amdgcn_rcpf(et[e]); PIN(et[e]); }
            } else if constexpr (st == 11) {
#pragma unroll
                for (int e = 0; e < 4; ++e) { eu[e] *= et[e]; PIN(eu[e]); }
            } else {
                eo[e0 >> 1] = pk(eu[0], eu[1]); eo[(e0 >> 1) + 1] = pk(eu[2], eu[3]);
                if (e0) st16(ws, l16, adst + t * 1024, eo); else { PIN(eo[0]); PIN(eo[1]); }
            }
        };
        auto ftile = [&](auto t_, lptr p) __attribute__((always_inline)) {
            constexpr int t = decltype(t_)::v;
            bf16x8 wf[4];
            wf[0] = lds128(p); wf[1] = lds128(p + 1024); wf[2] = lds128(p + 2048);
            SBAR();
            sfor<0, 32>([&](auto ks_) __attribute__((always_inline)) {
                constexpr int ks = decltype(ks_)::v;
                if (ks + 3 < 32) wf[(ks + 3) & 3] = lds128(p + (ks + 3) * 1024);
                epi(IC<(t - 3) * 32 + ks>{});
                SBAR();
                A[t & 3] = MFMA(wf[ks & 3], BC8(hf[ks]), ks ? A[t & 3] : f32x16{});
                SBAR();
            });
        };
        STAMPM(2, 15);
        LBAR();
        cg = *(LAS f32x4*)cs; cu = *(LAS f32x4*)(cs + 16);
        STAMP(11); STAMPM(3, 1);
        ftile(IC<3>{}, whi + 32768);
        STAMPM(3, 2);
        LBAR();
        STAMPM(3, 3);
        ftile(IC<4>{}, wlo);
        STAMPM(3, 4);
        LBAR();
        STAMPM(3, 5);
        ftile(IC<5>{}, wlo + S1);
        STAMPM(3, 6);
        LBAR();
        STAMPM(3, 7);
        ftile(IC<6>{}, whi);
        STAMPM(3, 8);
        asm volatile("s_waitcnt lgkmcnt(0)" ::: "memory");
        if (lane == 0) __hip_atomic_fetch_add((LAS unsigned*)(lds + M_CNT), 1u, __ATOMIC_RELAXED, __HIP_MEMORY_SCOPE_WORKGROUP);
        sfor<128, 14 * 13>(epi);
        STAMPM(3, 9);
        STAMP(12);
        f32x16 C[4];
        {
            u32x4 af[8][2];
            auto aoff = [&](int k, int t) { return WS_ACT + ((rb * 2 + t) * 112 + (4 * w + (k & 3)) * 7 + 5 + (k >> 2)) * 1024; };
#pragma unroll
            for (int k = 0; k < 8; ++k) { af[k][0] = ld16(ws, l16, aoff(k, 0)); af[k][1] = ld16(ws, l16, aoff(k, 1)); }
            lptr wa0 = lds + opaque(w * 4096 + l16), wa3 = lds + opaque(S3 + w * 4096 + l16);
            bf16x8 wf[8][2];
#pragma unroll
            for (int k = 0; k < 8; ++k) { lptr p = (k < 4 ? wa3 : wa0) + (k & 3) * 1024; wf[k][0] = lds128(p); wf[k][1] = lds128(p + 16384); }
            STAMPM(3, 10);
            VMWAIT(16);
            STAMPM(3, 11);
            publish(fl + FL_4 + t4 * 16 + j4, tag, lane);
            STAMP(13); STAMPM(3, 12);
#pragma unroll
            for (int k = 0; k < 8; ++k) {
                if ((k & 1) == 0) verify4(af[k][0], af[k][1], af[k + 1][0], af[k + 1][1], ws, l16, aoff(k, 0), aoff(k, 1), aoff(k + 1, 0), aoff(k + 1, 1), fl + FL_4 + rb * 32, 32, tag RC(5));
#pragma unroll
                for (int q = 0; q < 4; ++q) C[q] = MFMA(wf[k][q >> 1], BC8(af[k][q & 1]), k ? C[q] : f32x16{});
                SBAR();
            }
            STAMPM(3, 13);
#pragma unroll
            for (int q = 0; q < 4; ++q) {
                if (q != w) {
                    const int pc = (q - w - 1) & 3;
                    lptr p = pc == 0 ? wa3 : (pc == 1 ? wa3 + 16384 : wa0);
#pragma unroll
                    for (int g = 0; g < 4; ++g) *(LAS f32x4*)(p + g * 1024) = (f32x4){C[q][4 * g], C[q][4 * g + 1], C[q][4 * g + 2], C[q][4 * g + 3]};
                }
            }
        }
        f32x16 acc = w == 0 ? C[0] : (w == 1 ? C[1] : (w == 2 ? C[2] : C[3]));
        STAMPM(3, 14);
        LBAR();
        STAMPM(3, 15);
        {
            lptr lp1 = lds + opaque(S1 + w * 4096 + l16);
#pragma unroll
            for (int s = 0; s < 7; ++s) {
                lptr p;
                if (s < 4) p = lp1 + s * 16384;
                else { const int pc = s - 4, src_w = (w - pc - 1) & 3; p = lds + opaque(src_w * 4096 + l16 + (pc == 0 ? S3 : (pc == 1 ? S3 + 16384 : 0))); }
#pragma unroll
                for (int g = 0; g < 4; ++g) {
                    const f32x4 v = *(LAS f32x4*)(p + g * 1024);
#pragma unroll
                    for (int j = 0; j < 4; ++j) acc[4 * g + j] += v[j];
                }
            }
        }
        STAMP(14);
        char* p = out + (size_t)(ttile * 32 + r) * 1024 + (ctile * 32 + 4 * h) * 2;
#pragma unroll
        for (int g = 0; g < 4; ++g) {
            const unsigned a = hr[g >> 1][(g & 1) * 2], c = hr[g >> 1][(g & 1) * 2 + 1];
            *(u32x2*)(p + g * 16) = (u32x2){pk(acc[4 * g] + bflo(a), acc[4 * g + 1] + bfhi(a)), pk(acc[4 * g + 2] + bflo(c), acc[4 * g + 3] + bfhi(c))};
        }
        asm volatile(".Lpf_end1:");
        }
    }
    STAMP(15); STAMP_FLUSH(ws_);
}

extern "C" void kernel_launch(void* const* d_in, const int* in_sizes, int n_in, void* d_out, int out_size, void* d_ws, size_t ws_size,
                              hipStream_t stream) {
    (void)hipFuncSetAttribute((const void*)fused, hipFuncAttributeMaxDynamicSharedMemorySize, LDS_BYTES);
    fused<<<256, 512, LDS_BYTES, stream>>>((const char*)d_in[0], (const char*)d_in[1], (const char*)d_in[2], (const char*)d_in[4],
                                           (const char*)d_in[5], (const char*)d_in[6], (char*)d_out, (char*)d_ws);
}
```

```cpp
#define AMP 7
#define AMPC 0
#include <hip/hip_runtime.h>
#include <hip/hip_bf16.h>
#include <stdint.h>

typedef __attribute__((ext_vector_type(8))) short bf16x8;
typedef __attribute__((ext_vector_type(4))) short s16x4;
typedef __attribute__((ext_vector_type(16))) float f32x16;
typedef __attribute__((ext_vector_type(4))) float f32x4;
typedef __attribute__((ext_vector_type(2))) float f32x2;
typedef __attribute__((ext_vector_type(4))) unsigned u32x4;
typedef __attribute__((ext_vector_type(2))) unsigned u32x2;
typedef __attribute__((ext_vector_type(2))) __bf16 bf16x2;
typedef unsigned long long u64;
#define LAS __attribute__((address_space(3)))
#define GAS __attribute__((address_space(1)))
typedef LAS char* lptr;

#define MFMA(a, b, c) __builtin_amdgcn_mfma_f32_32x32x16_bf16(a, b, c, 0, 0, 0)
#define SBAR() __builtin_amdgcn_sched_barrier(0)
#define WAITBAR(N) asm volatile("s_waitcnt vmcnt(%0) lgkmcnt(0)\n\ts_barrier" :: "i"(N) : "memory")
#define LBAR() asm volatile("s_waitcnt lgkmcnt(0)\n\ts_barrier" ::: "memory")
#define VMWAIT(N) asm volatile("s_waitcnt vmcnt(%0)" :: "i"(N) : "memory")
#define VMCLR() __builtin_amdgcn_s_waitcnt(0x0F70)
#define RLX __ATOMIC_RELAXED, __HIP_MEMORY_SCOPE_AGENT

#define STAMP_DECL unsigned long long t0_ = 0, tx_ = 0, c0_ = 0, cx_ = 0
#define STAMP(idx) do { if ((idx) == 0) asm volatile("s_memrealtime %0\n\ts_memtime %1\n\ts_waitcnt lgkmcnt(0)" : "=s"(t0_), "=s"(c0_) :: "memory"); \
                        if ((idx) == AMP) asm volatile("s_memrealtime %0\n\ts_memtime %1\n\ts_waitcnt lgkmcnt(0)" : "=s"(tx_), "=s"(cx_) :: "memory"); } while (0)
#define STAMPM(m, idx) do {} while (0)
#define STAMP_FLUSH(ws) do { if (w == 0) { unsigned long long n_, e_; asm volatile("s_memrealtime %0\n\ts_waitcnt lgkmcnt(0)" : "=s"(e_) :: "memory"); \
    const unsigned long long d_ = AMPC ? (cx_ - c0_) / 20 : tx_ - t0_; \
    for (int i_ = 0; i_ < 1000000; ++i_) { asm volatile("s_memrealtime %0\n\ts_waitcnt lgkmcnt(0)" : "=s"(n_) :: "memory"); if (n_ >= e_ + d_) break; } } } while (0)
#define RCARG
#define RC(i)
#define RCADD(n) do {} while (0)
#define RCINC(i, n) do {} while (0)

constexpr int D = 512;
constexpr unsigned WS_Q = 0;
constexpr unsigned WS_K = WS_Q + (2u << 20);
constexpr unsigned WS_V = WS_K + (2u << 20);
constexpr unsigned WS_O = WS_V + (2u << 20);
constexpr unsigned WS_H = WS_O + (2u << 20);
constexpr unsigned WS_ACT = WS_H + (2u << 20);
constexpr unsigned WS_WO = WS_ACT + (7u << 20);
constexpr unsigned WS_WGU = WS_WO + (512u << 10);
constexpr unsigned WS_WD = WS_WGU + (3584u << 10);
constexpr unsigned WS_HS = WS_WD + (1792u << 10);
constexpr unsigned WS_CS = WS_HS + (256u << 10);
constexpr unsigned WS_FL = WS_CS + (256u << 10);
constexpr unsigned WS_END = WS_FL + 65536;
constexpr int FL_P = 0, FL_1 = 1024, FL_R = 2048, FL_2 = 3072, FL_3 = 5120, FL_4 = 6144, FL_E = 7168;
constexpr unsigned POISON = 0xFFFFFFFFu;
constexpr int S0 = 0, S1 = 32768, S2 = 65536, S3 = 98304, M0 = 131072, LDS_BYTES = 163840;
constexpr int M_Q = M0, M_X = M0, M_STG = M0 + 18432, M_CONST = M0 + 26624, M_WORD = M0 + 30720, M_CNT = M0 + 16384;
constexpr int P1_W = 65536, P1_SLOT = 24576;

__device__ __forceinline__ void glds3(const void* sbase, unsigned voff, unsigned ldsaddr) {
    asm volatile("s_mov_b32 m0, %2\n\ts_nop 0\n\tglobal_load_lds_dwordx4 %0, %1\n\tglobal_load_lds_dwordx4 %0, %1 offset:1024\n\tglobal_load_lds_dwordx4 %0, %1 offset:2048"
                 :: "v"(voff), "s"(sbase), "s"(ldsaddr) : "memory");
}
__device__ __forceinline__ void glds3_upper(int w, const void* sbase, unsigned voff, unsigned ldsaddr) {
    asm volatile("s_cmp_lt_u32 %3, 4\n\ts_cbranch_scc1 1f\n\ts_mov_b32 m0, %2\n\ts_nop 0\n\tglobal_load_lds_dwordx4 %0, %1\n\t"
                 "global_load_lds_dwordx4 %0, %1 offset:1024\n\tglobal_load_lds_dwordx4 %0, %1 offset:2048\n1:"
                 :: "v"(voff), "s"(sbase), "s"(ldsaddr), "s"(w) : "scc", "memory");
}
__device__ __forceinline__ void touch(unsigned& d, const char* p, int ix, int first, int n) {
    asm volatile("s_cmp_lt_u32 %2, %3\n\ts_cbranch_scc0 1f\n\ts_load_dword %0, %1, 0\n1:" : "+s"(d) : "s"(p), "s"(ix - first), "s"(n) : "scc", "memory");
}
__device__ __forceinline__ void gld2(const void* sbase, unsigned voff, unsigned ldsaddr) {
    asm volatile("s_mov_b32 m0, %2\n\ts_nop 0\n\tglobal_load_lds_dwordx4 %0, %1 sc1\n\tglobal_load_lds_dwordx4 %0, %1 offset:1024 sc1"
                 :: "v"(voff), "s"(sbase), "s"(ldsaddr) : "memory");
}
__device__ __forceinline__ void gld4(const void* sbase, unsigned voff, unsigned ldsaddr) {
    asm volatile("s_mov_b32 m0, %2\n\ts_nop 0\n\tglobal_load_lds_dwordx4 %0, %1 sc1\n\tglobal_load_lds_dwordx4 %0, %1 offset:1024 sc1\n\t"
                 "global_load_lds_dwordx4 %0, %1 offset:2048 sc1\n\tglobal_load_lds_dwordx4 %0, %1 offset:3072 sc1"
                 :: "v"(voff), "s"(sbase), "s"(ldsaddr) : "memory");
}
__device__ __forceinline__ void gld8(const char* sbase, unsigned voff, unsigned ldsaddr) {
    asm volatile("s_mov_b32 m0, %2\n\ts_nop 0\n\tglobal_load_lds_dwordx4 %0, %1 offset:-4096 sc1\n\tglobal_load_lds_dwordx4 %0, %1 offset:-3072 sc1\n\t"
                 "global_load_lds_dwordx4 %0, %1 offset:-2048 sc1\n\tglobal_load_lds_dwordx4 %0, %1 offset:-1024 sc1\n\t"
                 "global_load_lds_dwordx4 %0, %1 sc1\n\tglobal_load_lds_dwordx4 %0, %1 offset:1024 sc1\n\t"
                 "global_load_lds_dwordx4 %0, %1 offset:2048 sc1\n\tglobal_load_lds_dwordx4 %0, %1 offset:3072 sc1"
                 :: "v"(voff), "s"(sbase + 4096), "s"(ldsaddr + 4096) : "memory");
}
__device__ __forceinline__ bf16x8 lds128(lptr p) { return *(const LAS bf16x8*)p; }
__device__ __forceinline__ s16x4 ldstr(lptr p) { return __builtin_amdgcn_ds_read_tr16_b64_v4i16((LAS s16x4*)p); }
__device__ __forceinline__ bf16x8 cat(s16x4 a, s16x4 b) { return __builtin_shufflevector(a, b, 0, 1, 2, 3, 4, 5, 6, 7); }
__device__ __forceinline__ unsigned pk(float lo, float hi) { bf16x2 r = {(__bf16)lo, (__bf16)hi}; return __builtin_bit_cast(unsigned, r); }
__device__ __forceinline__ float dot2(unsigned a, unsigned b, float c) {
    return __builtin_amdgcn_fdot2_f32_bf16(__builtin_bit_cast(bf16x2, a), __builtin_bit_cast(bf16x2, b), c, false);
}
__device__ __forceinline__ float bflo(unsigned u) { return __uint_as_float(u << 16); }
__device__ __forceinline__ float bfhi(unsigned u) { return __uint_as_float(u & 0xffff0000u); }
constexpr unsigned ONES2 = 0x3F803F80u;
__device__ __forceinline__ void stat2(u32x4 u, float& s1, float& s2) {
#pragma unroll
    for (int i = 0; i < 4; ++i) { s1 = dot2(u[i], ONES2, s1); s2 = dot2(u[i], u[i], s2); }
}
__device__ __forceinline__ void stat1(bf16x8 f, float& s1) {
    u32x4 u = __builtin_bit_cast(u32x4, f);
#pragma unroll
    for (int i = 0; i < 4; ++i) s1 = dot2(u[i], ONES2, s1);
}
__device__ __forceinline__ unsigned opaque(unsigned x) { asm volatile("" : "+v"(x)); return x; }
template <int I> struct IC { static constexpr int v = I; };
template <int B, int E, class Fn> __device__ __forceinline__ void sfor(Fn&& f) { if constexpr (B < E) { f(IC<B>{}); sfor<B + 1, E>(f); } }
#define PIN(x) asm volatile("" : "+v"(x))
__device__ __forceinline__ int fresh_lane() {
    int x; asm volatile("v_mbcnt_lo_u32_b32 %0, -1, 0\n\tv_mbcnt_hi_u32_b32 %0, -1, %0" : "=v"(x)); return x;
}
__device__ __forceinline__ int rfl(int x) { return __builtin_amdgcn_readfirstlane(x); }
__device__ __forceinline__ float halfsum(float x) {
    auto r = __builtin_amdgcn_permlane32_swap(__float_as_uint(x), __float_as_uint(x), false, false);
    return __uint_as_float(r[0]) + __uint_as_float(r[1]);
}
typedef __amdgpu_buffer_rsrc_t rsrc_t;
__device__ __forceinline__ void st16(rsrc_t r, unsigned voff, unsigned soff, u32x4 v) {
    __builtin_amdgcn_raw_buffer_store_b128(v, r, voff, soff, 16);
    asm volatile("s_nop 1" :: "v"(v));
}
__device__ __forceinline__ u32x4 ld16(rsrc_t r, unsigned voff, unsigned soff) { return __builtin_amdgcn_raw_buffer_load_b128(r, voff, soff, 16); }
__device__ __forceinline__ void publish(GAS u64* f, u64 tag, int lane) { if (lane == 0) __hip_atomic_store(f, tag, RLX); }
__device__ __forceinline__ void poll(GAS u64* f, u64 tag) {
#pragma nounroll
    for (unsigned spins = 0; spins < (1u << 16); ++spins) {
        const u64 v = __hip_atomic_load(f, RLX);
        if (__all(v == tag)) break;
        __builtin_amdgcn_s_sleep(2);
    }
    asm volatile("" ::: "memory");
}
__device__ __forceinline__ void poll(GAS u64* f, int n, u64 tag, int lane) { poll(f + (lane & (n - 1)), tag); }
#define SL1 20
#define SL2 8
#define SL3 8
__device__ __forceinline__ unsigned mx(u32x4 v, unsigned m) { return max(max(v[2], v[3]), max(max(v[0], v[1]), m)); }
__device__ __forceinline__ void verify4(u32x4& a, u32x4& b, u32x4& c, u32x4& d, rsrc_t ws, unsigned voff, unsigned oa, unsigned ob, unsigned oc,
                                        unsigned od, GAS u64* flags, int n, u64 tag RCARG) {
    if (__builtin_expect(__any(mx(d, mx(c, mx(b, mx(a, 0u)))) == POISON), 0)) {
#pragma nounroll
        for (int t = 0;; ++t) {
            if (t == 32) poll(flags, n, tag, (int)(voff >> 4)); else __builtin_amdgcn_s_sleep(2);
            a = ld16(ws, voff, oa); b = ld16(ws, voff, ob); c = ld16(ws, voff, oc); d = ld16(ws, voff, od);
            VMCLR();
            RCADD(1);
            if (t == 32 || !__any(mx(d, mx(c, mx(b, mx(a, 0u)))) == POISON)) break;
        }
    }
}
__device__ __forceinline__ void poll128(GAS u64* f, u64 tag, int lane) { poll(f + lane, tag); poll(f + 64 + lane, tag); }
__device__ __forceinline__ bool tags2(u32x4 v, u64 tag) { return v[0] == (unsigned)tag && v[2] == (unsigned)tag && v[1] == (unsigned)(tag >> 32) && v[3] == (unsigned)(tag >> 32); }
#define BC8(x) __builtin_bit_cast(bf16x8, x)
#define BAR() asm volatile("s_barrier" ::: "memory")

__device__ __forceinline__ float sum8(float x) {
    x += __builtin_bit_cast(float, __builtin_amdgcn_mov_dpp(__builtin_bit_cast(int, x), 0xB1, 0xF, 0xF, true));
    x += __builtin_bit_cast(float, __builtin_amdgcn_mov_dpp(__builtin_bit_cast(int, x), 0x4E, 0xF, 0xF, true));
    x += __builtin_bit_cast(float, __builtin_amdgcn_mov_dpp(__builtin_bit_cast(int, x), 0x141, 0xF, 0xF, true));
    return x;
}
#define XWAIT(A, B, C, D, NL, NC) asm volatile("s_cmp_lt_u32 %4, 4\n\ts_cbranch_scc1 1f\n\ts_waitcnt vmcnt(" #NL ")\n\ts_branch 2f\n1:\n\ts_waitcnt vmcnt(" #NC ")\n2:" \
                                               : "+v"(A), "+v"(B), "+v"(C), "+v"(D) : "s"(w) : "scc", "memory")
template <class Side>
__device__ __forceinline__ void p1_norm(lptr lds, const char* xs, int lane, int w, int rot, Side side) {
    const int pos = lane & 7, row = 8 * w + (lane >> 3);
    const unsigned xo = row * 1024 + pos * 16;
    u32x4 xr[8];
#pragma unroll
    for (int c = 0; c < 8; ++c) asm volatile("global_load_dwordx4 %0, %1, %2" : "=v"(xr[c]) : "v"(xo), "s"(xs + ((c + rot) & 7) * 128) : "memory");
    side(-1);
    float s1 = 0.f, s2 = 0.f, t1 = 0.f, t2 = 0.f;
    XWAIT(xr[0], xr[1], xr[2], xr[3], 10, 4);
    stat2(xr[0], s1, s2); stat2(xr[1], t1, t2); stat2(xr[2], s1, s2); stat2(xr[3], t1, t2);
    PIN(s1); PIN(s2); PIN(t1); PIN(t2);
    XWAIT(xr[4], xr[5], xr[6], xr[7], 6, 0);
    stat2(xr[4], s1, s2); stat2(xr[5], t1, t2); stat2(xr[6], s1, s2); stat2(xr[7], t1, t2);
    s1 = sum8(s1 + t1); s2 = sum8(s2 + t2);
    const float m = s1 * (1.f / D);
    const float rs = __builtin_amdgcn_rsqf(fmaxf(s2 * (1.f / D) - m * m, 0.f) + 1e-5f), nb = -m * rs;
    const unsigned a = row * 128 + ((pos ^ ((row >> 1) & 7)) << 4);
#pragma unroll
    for (int c = 0; c < 8; ++c) {
        u32x4 y;
#pragma unroll
        for (int i = 0; i < 4; ++i) y[i] = pk(fmaf(bflo(xr[c][i]), rs, nb), fmaf(bfhi(xr[c][i]), rs, nb));
        *(LAS u32x4*)(lds + a + ((c + rot) & 7) * 8192) = y;
        side(c);
    }
}
__device__ __forceinline__ void p1_store(rsrc_t ws, int lane, int u, int e, int tile, int hq, u32x4 v) {
    const int head0 = hq * 4 + (u & 1) * 2;
    if (u < 4) st16(ws, lane * 16, (u < 2 ? WS_Q : WS_K) + ((head0 + e) * 64 + tile) * 1024, v);
    else st16(ws, ((lane >> 4) & 1) * 65536 + (lane >> 5) * 256 + (lane & 15) * 16, WS_V + (head0 * 64 + tile) * 1024 + e * 512, v);
}
template <int CG>
__device__ __forceinline__ void p1_consume(lptr lds, rsrc_t ws, int lane, int t, int start, int rb, int hq) {
    const int r = lane & 31, h = lane >> 5;
    const int row = 32 * t + r;
    const unsigned xa = row * 128 + ((h ^ ((row >> 1) & 7)) << 4);
    const int q = (lane & 15) >> 2, p = lane & 3, blk = (lane >> 4) & 1;
    unsigned wa[2];
#pragma unroll
    for (int i = 0; i < 2; ++i) wa[i] = P1_W + h * 3072 + q * 128 + (((i * 4 + blk * 2 + (p >> 1)) ^ (((q >> 1) & 1) << 2)) << 4) + (p & 1) * 8;
    f32x16 acc[3] = {};
    bf16x8 xf[4], wf[4][3];
    auto rdx = [&](int slot, unsigned a) { xf[slot] = lds128(lds + a); };
    auto rdw = [&](int slot, int i, lptr p0, lptr p1, int ks) {
        const int u = CG * 3 + i;
        lptr a = ((u & 1) ? p1 : p0) + (u >> 1) * 1024 + ks * 6144;
        wf[slot][i] = cat(ldstr(a), ldstr(a + 512));
    };
    unsigned xc = (start & 7) * 8192;
    lptr c0 = lds + opaque(wa[0]), c1 = lds + opaque(wa[1]);
    LBAR();
#pragma unroll
    for (int ks = 0; ks < 2; ++ks) { rdx(ks, opaque(xc + (xa ^ (ks << 5)))); rdw(ks, 0, c0, c1, ks); rdw(ks, 1, c0, c1, ks); rdw(ks, 2, c0, c1, ks); }
    SBAR();
#pragma unroll 1
    for (int ch = 0; ch < 8; ++ch) {
        const unsigned xn = ((ch + 1 + start) & 7) * 8192, sn = ((ch + 1) & 3) * P1_SLOT;
        lptr n0 = lds + opaque(sn + wa[0]), n1 = lds + opaque(sn + wa[1]);
        const unsigned xa2 = opaque(xc + (xa ^ 64)), xa3 = opaque(xc + (xa ^ 96)), xb0 = opaque(xn + xa), xb1 = opaque(xn + (xa ^ 32));
#pragma unroll
        for (int ks = 0; ks < 4; ++ks) {
            const int s = (ks + 2) & 3;
            if (ks == 2 && ch < 7) LBAR();
#pragma unroll
            for (int i = 0; i < 3; ++i) {
                if (i == 0) rdx(s, ks == 0 ? xa2 : (ks == 1 ? xa3 : (ks == 2 ? xb0 : xb1)));
                if (ks < 2) rdw(s, i, c0, c1, ks + 2); else rdw(s, i, n0, n1, ks - 2);
                SBAR();
                acc[i] = (CG * 3 + i >= 4) ? MFMA(xf[ks], wf[ks][i], acc[i]) : MFMA(wf[ks][i], xf[ks], acc[i]);
                SBAR();
            }
        }
        xc = xn; c0 = n0; c1 = n1;
    }
#pragma unroll
    for (int i = 0; i < 3; ++i) {
        const float sc = (CG * 3 + i < 2) ? 0.25f * 1.4426950408889634f : 1.f;
#pragma unroll
        for (int e = 0; e < 2; ++e) {
            u32x4 v;
#pragma unroll
            for (int j = 0; j < 4; ++j) v[j] = pk(acc[i][8 * e + 2 * j] * sc, acc[i][8 * e + 2 * j + 1] * sc);
            p1_store(ws, lane, CG * 3 + i, e, rb * 2 + t, hq, v);
        }
    }
}

#define A_EXP(d, s) asm volatile("v_exp_f32 %0, %1" : "=v"(d) : "v"(s))
#define A_CVT(d, a, b) asm volatile("v_cvt_pk_bf16_f32 %0, %1, %2" : "=v"(d) : "v"(a), "v"(b))
#define A_QK(d, k, q) asm volatile("v_mfma_f32_32x32x16_bf16 %0, %1, %2, 0" : "=&v"(d) : "v"(k), "v"(q))
#define A_PV(o, v, p) asm volatile("v_mfma_f32_32x32x16_bf16 %0, %1, %2, %0" : "+v"(o) : "v"(v), "v"(p))
#define UNIT(SC, SN, KN, QN, OPREV, VPREV1, OC, VC0, LOADS) do { \
        float e0, e1, e2, e3, e4, e5, e6, e7; u32x4 p0; \
        A_QK(SN, KN, QN); \
        A_EXP(e0, SC[0]); A_EXP(e1, SC[1]); \
        A_PV(OPREV, VPREV1, p1); \
        LOADS; SBAR(); \
        A_EXP(e2, SC[2]); A_EXP(e3, SC[3]); A_CVT(p0[0], e0, e1); \
        A_EXP(e4, SC[4]); A_EXP(e5, SC[5]); A_CVT(p0[1], e2, e3); \
        A_EXP(e6, SC[6]); A_EXP(e7, SC[7]); A_CVT(p0[2], e4, e5); \
        A_EXP(e0, SC[8]); A_CVT(p0[3], e6, e7); A_EXP(e1, SC[9]); \
        A_EXP(e2, SC[10]); A_PV(OC, VC0, p0); A_EXP(e3, SC[11]); A_CVT(p1[0], e0, e1); \
        A_EXP(e4, SC[12]); A_EXP(e5, SC[13]); A_CVT(p1[1], e2, e3); \
        A_EXP(e6, SC[14]); A_EXP(e7, SC[15]); A_CVT(p1[2], e4, e5); \
        asm volatile("s_nop 0"); A_CVT(p1[3], e6, e7); \
    } while (0)

template <class Hook, class Gap>
__device__ __forceinline__ void attn_main(lptr lds, unsigned ka, unsigned va, unsigned vstep, bf16x8 q0, bf16x8 q1, f32x16& o0, f32x16& o1, Hook hook, Gap gap) {
    f32x16 sa, sb;
    u32x4 p1 = {0u, 0u, 0u, 0u};
    bf16x8 kc, kn, va0, va1, vb0, vb1;
    kc = lds128(lds + ka); va0 = lds128(lds + va); va1 = lds128(lds + va + 512);
    vb1 = va1;
    A_QK(sa, kc, q0);
    asm volatile("s_nop 7\n\ts_nop 7" : "+v"(sa), "+v"(p1), "+v"(o0), "+v"(o1));
    auto settle_a = [&]() { asm volatile("s_nop 3" : "+v"(o0), "+v"(o1), "+v"(sa)); };
    auto settle_b = [&]() { asm volatile("s_nop 3" : "+v"(o0), "+v"(o1), "+v"(sb)); };
#pragma unroll
    for (int c = 0; c < 8; ++c) {
        __builtin_amdgcn_s_setprio(3);
        gap(c, 0, settle_a);
        UNIT(sa, sb, kc, q1, o1, vb1, o0, va0, (kn = lds128(lds + ka + 1024), hook(c, 0)));
        gap(c, 1, settle_b);
        UNIT(sb, sa, kn, q0, o0, va1, o1, va0, (vb0 = lds128(lds + va + 1024), vb1 = lds128(lds + va + 1536), hook(c, 1)));
        __builtin_amdgcn_s_setprio(2);
        gap(c, 2, settle_a);
        UNIT(sa, sb, kn, q1, o1, va1, o0, vb0, (kc = lds128(lds + ka + 2048), hook(c, 2)));
        gap(c, 3, settle_b);
        UNIT(sb, sa, kc, q0, o0, vb1, o1, vb0, (va0 = lds128(lds + va + 2048), va1 = lds128(lds + va + 2560), hook(c, 3)));
        __builtin_amdgcn_s_setprio(1);
        gap(c, 4, settle_a);
        UNIT(sa, sb, kc, q1, o1, vb1, o0, va0, (kn = lds128(lds + ka + 3072), hook(c, 4)));
        gap(c, 5, settle_b);
        UNIT(sb, sa, kn, q0, o0, va1, o1, va0, (vb0 = lds128(lds + va + 3072), vb1 = lds128(lds + va + 3584), hook(c, 5)));
        __builtin_amdgcn_s_setprio(0);
        if (c < 7) { ka += 4096; va += vstep; }
        gap(c, 6, settle_a);
        UNIT(sa, sb, kn, q1, o1, va1, o0, vb0, (kc = lds128(lds + ka), hook(c, 6)));
        gap(c, 7, settle_b);
        UNIT(sb, sa, kc, q0, o0, vb1, o1, vb0, (va0 = lds128(lds + va), va1 = lds128(lds + va + 512), hook(c, 7)));
    }
    asm volatile("s_nop 1\n\tv_mfma_f32_32x32x16_bf16 %0, %2, %3, %0\n\ts_nop 7\n\ts_nop 7" : "+v"(o1), "+v"(sa) : "v"(vb1), "v"(p1));
    asm volatile("" : "+v"(o0));
}

__device__ __forceinline__ void pexp(const f32x16& s, float m, bf16x8& p0, bf16x8& p1) {
    float e[16];
#pragma unroll
    for (int i = 0; i < 16; ++i) e[i] = __builtin_amdgcn_exp2f(s[i] - m);
    p0 = __builtin_bit_cast(bf16x8, (u32x4){pk(e[0], e[1]), pk(e[2], e[3]), pk(e[4], e[5]), pk(e[6], e[7])});
    p1 = __builtin_bit_cast(bf16x8, (u32x4){pk(e[8], e[9]), pk(e[10], e[11]), pk(e[12], e[13]), pk(e[14], e[15])});
}

__global__ __launch_bounds__(512, 2) void fused(const char* __restrict__ x, const char* __restrict__ wqkv, const char* __restrict__ wo,
                                                const char* __restrict__ wg, const char* __restrict__ wu, const char* __restrict__ wd,
                                                char* __restrict__ out, char* ws_) {
    extern __shared__ __attribute__((aligned(16))) char smem[];
    lptr lds = (lptr)smem;
    const unsigned lb = (unsigned)(size_t)lds;
    STAMP_DECL; STAMP(0);
    const int tid = threadIdx.x, lane = tid & 63, w = rfl(tid >> 6);
    const int b = blockIdx.x, xcd = b & 7, li = b >> 3;
    const int r = lane & 31, h = lane >> 5;
    const unsigned l16 = lane * 16;
    GAS u64* fl = (GAS u64*)(ws_ + WS_FL);
    const rsrc_t ws = __builtin_amdgcn_make_buffer_rsrc(ws_, 0, WS_END, 0x00020000);
    const int rb1 = (xcd >> 1) * 8 + (li >> 2), hq = (xcd & 1) * 4 + (li & 3);
    const int head = xcd * 4 + (li & 3), qb = li >> 2;
    const int rb = xcd * 4 + (li >> 3), cb = li & 7;
    const int g4 = (xcd >> 2) * 8 + (li >> 2), j4 = (xcd & 3) * 4 + (li & 3);
    const int start1 = ((li >> 2) + (li & 3) * 2 + xcd) & 7;
    const unsigned woff = (8 * (w & 3) + (lane >> 3)) * 3072 + (((lane & 7) ^ ((lane >> 2) & 4)) << 4);
    const char* wsrc = wqkv + hq * 128;
    auto issue = [&](int s) {
        glds3(wsrc + (size_t)((s + 2 * start1) & 15) * 32 * 3072, woff, lb + P1_W + (s & 7) * 12288 + (w & 3) * 3072);
    };
    unsigned tlbw = 0;
    auto issue_l = [&](int s) {
        glds3_upper(w, wsrc + (size_t)((s + 2 * start1) & 15) * 32 * 3072, woff, lb + P1_W + (s & 7) * 12288 + (w & 3) * 3072);
    };
    p1_norm(lds, x + (size_t)rb1 * 65536, lane, w, (li & 3) * 2, [&](int c) __attribute__((always_inline)) {
        if (c < 0) {
            issue_l(0); issue_l(1);
            const int ix = li * 8 + w;
            const char* pc; int cl0, cl1;
            asm volatile("s_getpc_b64 %0\n.Lpf_here:\n\ts_mov_b32 %1, .Lpf_end0-.Lpf_here\n\ts_mov_b32 %2, .Lpf_end1-.Lpf_here" : "=s"(pc), "=s"(cl0), "=s"(cl1));
            const int clen = cl0 > cl1 ? cl0 : cl1;
#pragma unroll
            for (int k = 0; k < 6; ++k) touch(tlbw, pc + (ix + 256 * k) * 64, (ix + 256 * k) * 64 + 4, 0, clen);
            touch(tlbw, ws_ + (size_t)ix * 262144, ix, 0, 96);
            touch(tlbw, wg + (size_t)(ix - 96) * 65536, ix, 96, 28); touch(tlbw, wu + (size_t)(ix - 124) * 65536, ix, 124, 28);
            touch(tlbw, wd + (size_t)(ix - 152) * 65536, ix, 152, 28); touch(tlbw, wo + (size_t)(ix - 180) * 65536, ix, 180, 8);
            touch(tlbw, out + (size_t)(ix - 188) * 65536, ix, 188, 32);
        } else if (c == 2) issue_l(2); else if (c == 6) issue_l(3);
    });
    asm volatile("s_waitcnt lgkmcnt(0)" : "+s"(tlbw) :: "memory");
    STAMP(7); STAMPM(1, 1);
    u64 e_raw = __hip_atomic_load(fl + FL_E, RLX);
    asm volatile("" :: "s"(wo), "s"(wg), "s"(wu), "s"(wd), "s"(out));
    const int qp = w & 3, kh = w >> 2;
    const unsigned ka0 = S0 + kh * 32768 + l16;
    unsigned va0, vstep;
    if (r < 16) { va0 = S2 + kh * 32768 + h * 256 + r * 16; vstep = 4096; }
    else { va0 = M_CONST + ((r >= 20 && r < 28) ? (r == 20 ? 64 : 80) : (r == 16 ? 0 : 16)); vstep = 0; }

    const u32x4 poison = {POISON, POISON, POISON, POISON};
    const char* ksrc = ws_ + WS_K + (size_t)head * 65536;

    GAS u64* myf2 = fl + FL_2 + (qb * 8 + qp * 2 + kh) * 32 + head;
    auto attn_finish = [&](f32x16& o0, f32x16& o1, bf16x8 q0, bf16x8 q1, u64 tag) __attribute__((always_inline)) {
    STAMP(5); STAMPM(4, 15);
    const int lane = fresh_lane();
    {
        LAS float* xo = (LAS float*)(lds + M_X + w * 2304);
        LAS float* xi = (LAS float*)(lds + M_X + (w ^ 4) * 2304);
        const unsigned odst = WS_O + ((qb * 8 + qp * 2 + kh) * 32 + head) * 1024;
        float mag = 0.f;
#pragma unroll
        for (int i = 0; i < 8; ++i) mag = fmaf(fabsf(o0[i]), 0.0625f, fmaf(fabsf(o1[i]), 0.0625f, mag));
        const bool ok = (mag <= 5.0e36f) & (o0[8] >= 0x1p-31f) & (o0[8] <= 0x1p99f) & (o1[8] >= 0x1p-31f) & (o1[8] <= 0x1p99f);
        if (!ok) *(LAS unsigned*)(lds + M_WORD) = 1u;
#pragma unroll
        for (int i = 0; i < 9; ++i) xo[i * 64 + lane] = kh ? o0[i] : o1[i];
        VMCLR();
        STAMPM(2, 1);
        LBAR();
        STAMPM(2, 2);
        float o[9];
#pragma unroll
        for (int i = 0; i < 9; ++i) o[i] = (kh ? o1[i] : o0[i]) + xi[i * 64 + lane];
        if (rfl(*(LAS unsigned*)(lds + M_WORD))) {
            LBAR();
            const f32x16 zero = {};
            float m0 = -INFINITY, m1 = -INFINITY;
            unsigned ka = ka0, va = va0;
#pragma unroll 1
            for (int i = 0; i < 32; ++i) {
                const bf16x8 kf = lds128(lds + ka);
                const f32x16 s0 = MFMA(kf, q0, zero), s1 = MFMA(kf, q1, zero);
#pragma unroll
                for (int j = 0; j < 16; ++j) { m0 = fmaxf(m0, s0[j]); m1 = fmaxf(m1, s1[j]); }
                ka += 1024;
            }
            m0 = fmaxf(m0, __shfl_xor(m0, 32)); m1 = fmaxf(m1, __shfl_xor(m1, 32));
            xo[lane] = m0; xo[64 + lane] = m1;
            LBAR();
            m0 = fmaxf(m0, xi[lane]); m1 = fmaxf(m1, xi[64 + lane]);
            LBAR();
            o0 = zero; o1 = zero; ka = ka0;
#pragma unroll 1
            for (int i = 0; i < 32; ++i) {
                const unsigned vo = va + (i & 3) * 1024;
                const bf16x8 kf = lds128(lds + ka), v0 = lds128(lds + vo), v1 = lds128(lds + vo + 512);
                bf16x8 pa, pb;
                pexp(MFMA(kf, q0, zero), m0, pa, pb); o0 = MFMA(v0, pa, o0); o0 = MFMA(v1, pb, o0);
                SBAR();
                pexp(MFMA(kf, q1, zero), m1, pa, pb); o1 = MFMA(v0, pa, o1); o1 = MFMA(v1, pb, o1);
                ka += 1024; if ((i & 3) == 3) va += vstep;
            }
#pragma unroll
            for (int i = 0; i < 9; ++i) xo[i * 64 + lane] = kh ? o0[i] : o1[i];
            LBAR();
#pragma unroll
            for (int i = 0; i < 9; ++i) o[i] = (kh ? o1[i] : o0[i]) + xi[i * 64 + lane];
        }
        const float inv = __builtin_amdgcn_rcpf(o[8]);
        st16(ws, l16, odst, (u32x4){pk(o[0] * inv, o[1] * inv), pk(o[2] * inv, o[3] * inv), pk(o[4] * inv, o[5] * inv), pk(o[6] * inv, o[7] * inv)});
    }
    if (b == 0 && w == 0) publish(fl + FL_E, tag, lane);
    STAMP(6); STAMPM(2, 3);
    };
    if (w >= 4) {
        f32x16 o0 = {}, o1 = {};
        bf16x8 q0, q1;
        u32x4 stash[32];
        u64 tag;
        f32x4 csA, csB;
        const int l = w - 4;
        u32x4 jr[2][4];
        WAITBAR(7); STAMPM(1, 2); issue(4); issue(5); issue(6); issue(7); STAMPM(1, 3);
        WAITBAR(12); STAMPM(1, 4); issue(8); issue(9); STAMPM(1, 5);
        WAITBAR(12); STAMPM(1, 6); issue(10); issue(11);
        WAITBAR(12); STAMPM(1, 7); issue(12); issue(13);
        WAITBAR(12); STAMPM(1, 8); issue(14); issue(15); STAMPM(1, 9);
        int bj = b;
        asm volatile("" : "+s"(bj));
        const int krow = lane >> 3, ch = lane & 7;
        const bool isg = bj < 112, isd = bj < 224;
        const int j0 = (isd ? bj - 112 : bj - 224) * 8 + 2 * l;
        const int nks = (isd && !isg) ? 112 : 32;
        const int lc = isg ? bj >> 2 : (isd ? j0 / 112 : j0 >> 5), ks0 = isg ? (bj & 3) * 8 + 2 * l : j0 - lc * nks;
        const unsigned rstride = isg ? 3584 : 1024;
        const unsigned jo = (ks0 * 16 + krow) * rstride + lc * 128 + ch * 16;
        const char* jsrc = isg ? wg : (isd ? wd : wo);
        const unsigned jbase = isg ? WS_WGU + lc * 4 * 32768 : (isd ? WS_WD : WS_WO) + lc * 2 * nks * 1024;
        const bool perm = isd && !isg;
        const unsigned jdst0 = jbase + (perm ? (ks0 % 7) * 16 + ks0 / 7 : ks0) * 1024, jdst1 = jbase + (perm ? ((ks0 + 1) % 7) * 16 + (ks0 + 1) / 7 : ks0 + 1) * 1024;
        const unsigned jvo = isg ? l16 : WS_END;
        {
            const rsrc_t ra = __builtin_amdgcn_make_buffer_rsrc((void*)jsrc, 0, isd ? 1835008 : 524288, 0x00020000);
            const rsrc_t rb2 = __builtin_amdgcn_make_buffer_rsrc((void*)wu, 0, isg ? 1835008 : 0, 0x00020000);
#pragma unroll
            for (int j = 0; j < 2; ++j) {
                jr[j][0] = __builtin_amdgcn_raw_buffer_load_b128(ra, jo + j * 16 * rstride, 0, 2); jr[j][1] = __builtin_amdgcn_raw_buffer_load_b128(ra, jo + (j * 16 + 8) * rstride, 0, 2);
                jr[j][2] = __builtin_amdgcn_raw_buffer_load_b128(rb2, jo + j * 16 * rstride, 0, 2); jr[j][3] = __builtin_amdgcn_raw_buffer_load_b128(rb2, jo + (j * 16 + 8) * rstride, 0, 2);
            }
        }
        STAMPM(1, 10); STAMPM(5, 1);
        WAITBAR(20); STAMPM(1, 11); WAITBAR(14); STAMPM(1, 12); WAITBAR(8); STAMPM(1, 13);
            LBAR();
        STAMP(1); STAMPM(1, 14); STAMPM(5, 2);
        VMCLR();
        STAMPM(5, 3);
        PIN(e_raw);
        tag = ((u64)(unsigned)rfl((int)(e_raw >> 32)) << 32 | (unsigned)rfl((int)e_raw)) + 1;
        if (l == 1) {
            const unsigned v = ((lane & 3) == 0) ? ONES2 : 0u;
            *(LAS u32x4*)(lds + M_CONST + (lane >> 3) * 512 + (lane & 7) * 16) = (u32x4){v, v, v, v};
            if (lane == 0) *(LAS unsigned*)(lds + M_WORD) = 0u;
        }
        u32x4 tf[8];
        f32x16 csa = {};
        {
            lptr st = lds + M_STG + l * 2048;
            lptr ta = st + (4 * h + ((lane & 15) >> 2)) * 64 + ((lane >> 4) & 1) * 32 + (lane & 3) * 8;
            if (isg) {
                lptr sw = st + krow * 64 + ((ch >> 1) & 1) * 1024 + (ch & 1) * 16;
#pragma unroll
                for (int i = 0; i < 4; ++i) {
                    const int j = i >> 1, rr = i & 1;
                    if ((ch >> 2) == rr) {
                        *(LAS u32x4*)sw = jr[j][0]; *(LAS u32x4*)(sw + 512) = jr[j][1];
                        *(LAS u32x4*)(sw + 32) = jr[j][2]; *(LAS u32x4*)(sw + 512 + 32) = jr[j][3];
                    }
#pragma unroll
                    for (int k = 0; k < 2; ++k) {
                        const bf16x8 f = cat(ldstr(ta + k * 1024), ldstr(ta + k * 1024 + 512));
                        const unsigned sel = (r == 2 * rr + k) ? ONES2 : 0u;
                        csa = MFMA(BC8(((u32x4){sel, sel, sel, sel})), f, csa);
                        tf[j * 4 + 2 * rr + k] = __builtin_bit_cast(u32x4, f);
                    }
                }
            } else {
                lptr sw = st + krow * 64 + (ch >> 2) * 1024 + (ch & 3) * 16;
#pragma unroll
                for (int j = 0; j < 2; ++j) {
                    *(LAS u32x4*)sw = jr[j][0]; *(LAS u32x4*)(sw + 512) = jr[j][1];
#pragma unroll
                    for (int k = 0; k < 2; ++k) {
                        tf[j * 4 + k] = __builtin_bit_cast(u32x4, cat(ldstr(ta + k * 1024), ldstr(ta + k * 1024 + 512)));
                        tf[j * 4 + 2 + k] = tf[j * 4 + k];
                    }
                }
            }
        }
        const u32x4 csv = {__float_as_uint(csa[0]), __float_as_uint(csa[1]), __float_as_uint(csa[2]), __float_as_uint(csa[3])};
        if (l == 0) {
            STAMPM(5, 4);
            __builtin_amdgcn_s_sleep(SL1);
            STAMPM(5, 5);
            poll(fl + FL_1 + (head >> 2) * 128 + qb * 16, 16, tag, lane);
            STAMP(4); STAMPM(5, 6);
            gld8(ksrc - (WS_K - WS_Q) + qb * 8192, l16, lb + M_Q);
            gld4(ksrc + qb * 8192, l16, lb + S0); gld4(ksrc + qb * 8192 + 4096, l16, lb + S1);
            gld4(ksrc + (WS_V - WS_K) + qb * 8192, l16, lb + S2); gld4(ksrc + (WS_V - WS_K) + qb * 8192 + 4096, l16, lb + S3);
        }
        u32x4 cp[8];
        u64 fa, fc;
        STAMPM(5, 7);
        WAITBAR(0);
        STAMP(3); STAMPM(5, 8);
        q0 = lds128(lds + M_Q + qp * 2048 + l16); q1 = lds128(lds + M_Q + qp * 2048 + 1024 + l16);
        const unsigned sdo = WS_WD + (cb * 2 * 112 + 4 * l) * 1024;
        GAS u64* fra = fl + FL_R + 4 * (112 + 14 * cb) + (lane < 56 ? lane : 55);
        GAS u64* frc = fl + FL_R + 16 * ((7 * j4) >> 2) + (lane < 16 * (((7 * j4 + 6) >> 2) - ((7 * j4) >> 2) + 1) ? lane : 0);
        attn_main(lds, ka0, va0, vstep, q0, q1, o0, o1, [&](int c, int i) __attribute__((always_inline)) {
            const int n = c * 8 + i;
            if (n >= 1 && n < 17 && (n & 1) == 1) {
                const int m = n >> 1, k = m & 3;
                st16(ws, k < 2 ? l16 : jvo, ((m >> 2) ? jdst1 : jdst0) + k * nks * 1024, tf[m]);
            }
            if (n == 16) st16(ws, (isg && h == 0) ? r * 16 : WS_END, WS_CS + (((b & 3) * 4 + l) * 28 + lc) * 512, csv);
            if (n == 32) { fa = __hip_atomic_load(fra, RLX); fc = __hip_atomic_load(frc, RLX); }
            if (n >= 38 && n < 54) {
                const int m = n - 38;
                stash[2 * m] = ld16(ws, l16, sdo + ((m >> 2) * 16 + (m & 3)) * 1024);
                stash[2 * m + 1] = ld16(ws, l16, sdo + (112 + (m >> 2) * 16 + (m & 3)) * 1024);
            }
            if (n == 35) {
#pragma unroll
                for (int k = 0; k < 4; ++k) {
                    const unsigned so = WS_CS + ((4 * l + k) * 28 + ((7 * j4) >> 2)) * 512;
                    cp[k] = ld16(ws, l16, so); cp[4 + k] = ld16(ws, r * 16, so + 1024);
                }
            }
        }, [&](int c, int i, auto settle) __attribute__((always_inline)) {
            const int n = c * 8 + i;
            if (n == 2) STAMPM(4, 1); if (n == 6) STAMPM(4, 2); if (n == 7) STAMPM(4, 3); if (n == 14) STAMPM(4, 4); if (n == 15) STAMPM(4, 5); if (n == 20) STAMPM(4, 6); if (n == 21) STAMPM(4, 7);
            if (n == 24) STAMPM(4, 8); if (n == 32) STAMPM(4, 9); if (n == 36) STAMPM(4, 10); if (n == 40) STAMPM(4, 11); if (n == 48) STAMPM(4, 12); if (n == 56) STAMPM(4, 13); if (n == 60) STAMPM(4, 14);
            if (n == 6 || n == 14) {
                BAR();
            } else if (n == 20) {
                settle();
                VMWAIT(0);
                publish(fl + FL_R + b * 4 + l, tag, lane);
            } else if (n == 35) {
                settle();
                PIN(fa); PIN(fc);
                if (!__all(fa == tag && fc == tag)) { RCINC(6, 1); poll(fra, tag); poll(frc, tag); }
            } else if (n == 38) {
                asm volatile("" : "+v"(cp[0]), "+v"(cp[1]), "+v"(cp[2]), "+v"(cp[3]), "+v"(cp[4]), "+v"(cp[5]), "+v"(cp[6]), "+v"(cp[7]));
                csA = (__builtin_bit_cast(f32x4, cp[0]) + __builtin_bit_cast(f32x4, cp[1])) + (__builtin_bit_cast(f32x4, cp[2]) + __builtin_bit_cast(f32x4, cp[3]));
                csB = (__builtin_bit_cast(f32x4, cp[4]) + __builtin_bit_cast(f32x4, cp[5])) + (__builtin_bit_cast(f32x4, cp[6]) + __builtin_bit_cast(f32x4, cp[7]));
                PIN(csA); PIN(csB);
            }
        });
        attn_finish(o0, o1, q0, q1, tag);
        const char* gu = ws_ + WS_WGU + (size_t)j4 * 7 * 32768 + l * 8192;
        const char* wdn = ws_ + WS_WD + (size_t)((cb * 2 + (l >> 1)) * 112 + 80) * 1024 + (l & 1) * 8192;
        const unsigned lq = lb + l * 8192;
        {
            gld8(gu, l16, lq + S0);
            STAMPM(2, 4);
            __builtin_amdgcn_s_sleep(SL2);
            STAMP(7);
            const char* os = ws_ + WS_O + (size_t)(rb * 2 + (l >> 1)) * 32768 + (l & 1) * 4096;
            const unsigned lo = lb + S3 + l * 4096;
            gld4(os, l16, lo); gld4(os + 8192, l16, lo + 16384);
            STAMPM(2, 5);
            WAITBAR(4);
            STAMPM(2, 6);
            gld4(os + 16384, l16, lo + 32768); gld4(os + 24576, l16, lo + 49152);
            publish(myf2, tag, lane);
            STAMPM(2, 7);
            WAITBAR(9); STAMPM(2, 8); gld8(gu + 32768, l16, lq + S1); STAMPM(2, 9);
            WAITBAR(13);
            WAITBAR(9);
        }
        LBAR();
        STAMPM(2, 10);
        gld8(gu + 2 * 32768, l16, lq + S2); gld8(gu + 3 * 32768, l16, lq + S3);
        STAMPM(2, 11);
        if (l == 0 && lane == 0) *(LAS unsigned*)(lds + M_CNT) = 0u;
        *(LAS f32x4*)(lds + M0 + 1024 + 2048 * l + l16) = csA; *(LAS f32x4*)(lds + M0 + 1024 + 2048 * l + 1024 + l16) = csB;
        STAMPM(2, 12);
        WAITBAR(8);
        {
            const int ft = ((7 * j4) & 3) + 2 * l + h;
            LAS float* p = (LAS float*)(lds + M0 + 1024 + (ft >> 2) * 512 + r * 16 + (ft & 3) * 4);
            *(LAS float*)(lds + M0 + (l * 64 + lane) * 4) = (p[0] + p[512]) + (p[1024] + p[1536]);
        }
        WAITBAR(0);
        STAMPM(3, 1);
        gld8(gu + 4 * 32768, l16, lq + S0); gld8(gu + 5 * 32768, l16, lq + S1); gld8(gu + 6 * 32768, l16, lq + S2);
        STAMPM(3, 2);
        WAITBAR(16); STAMPM(3, 3); gld8(wdn, l16, lq + S3); STAMPM(3, 4);
        WAITBAR(16); STAMPM(3, 5); gld8(wdn + 16384, l16, lq + S0); STAMPM(3, 6);
        WAITBAR(0); STAMPM(3, 7);
        f32x16 C[4];
        u32x4 af[20][2], wx[8];
        constexpr int AH = 6;
        auto aoff = [&](int k, int t) { return WS_ACT + ((rb * 2 + t) * 112 + (4 * l + (k & 3)) * 7 + (k >> 2)) * 1024; };
#pragma unroll
        for (int k = 0; k < AH; ++k) { af[k][0] = ld16(ws, l16, aoff(k, 0)); af[k][1] = ld16(ws, l16, aoff(k, 1)); }
        SBAR();
#pragma unroll
        for (int k = 0; k < 20; ++k) {
            if (k == 0) STAMPM(3, 9);
            if ((k & 1) == 0) verify4(af[k][0], af[k][1], af[k + 1][0], af[k + 1][1], ws, l16, aoff(k, 0), aoff(k, 1), aoff(k + 1, 0), aoff(k + 1, 1), fl + FL_4 + rb * 32, 32, tag RC(5));
            if (k == 0) STAMPM(3, 10); if (k == 4) STAMPM(3, 11); if (k == 8) STAMPM(3, 12); if (k == 16) STAMPM(3, 13);
#pragma unroll
            for (int q = 0; q < 4; ++q) C[q] = MFMA(BC8(k < 16 ? stash[2 * k + (q >> 1)] : wx[2 * (k - 16) + (q >> 1)]), BC8(af[k][q & 1]), k ? C[q] : f32x16{});
            SBAR();
            if (k + AH < 20) { af[k + AH][0] = ld16(ws, l16, aoff(k + AH, 0)); af[k + AH][1] = ld16(ws, l16, aoff(k + AH, 1)); }
            if (k >= 6 && k < 10) {
                const unsigned wo4 = WS_WD + (cb * 2 * 112 + 64 + 4 * l + k - 6) * 1024;
                wx[2 * (k - 6)] = ld16(ws, l16, wo4); wx[2 * (k - 6) + 1] = ld16(ws, l16, wo4 + 112 * 1024);
            }
            SBAR();
        }
        STAMP(12); STAMPM(3, 14);
        if (l >= 2) {
#pragma nounroll
            for (int t = 0; t < (1 << 20); ++t) {
                if (rfl(__hip_atomic_load((LAS unsigned*)(lds + M_CNT), __ATOMIC_RELAXED, __HIP_MEMORY_SCOPE_WORKGROUP)) == 4) break;
                __builtin_amdgcn_s_sleep(1);
            }
            asm volatile("" ::: "memory");
        }
        lptr pp = lds + opaque(S1 + l * 16384 + l16);
#pragma unroll
        for (int q = 0; q < 4; ++q)
#pragma unroll
            for (int g = 0; g < 4; ++g) *(LAS f32x4*)(pp + q * 4096 + g * 1024) = (f32x4){C[q][4 * g], C[q][4 * g + 1], C[q][4 * g + 2], C[q][4 * g + 3]};
        STAMP(14);
        LBAR();
        asm volatile(".Lpf_end0:");
    } else {
        f32x16 o0 = {}, o1 = {};
        bf16x8 q0, q1;
        u32x4 stash[32];
        u64 tag;
        if (w >> 1) p1_consume<1>(lds, ws, lane, w & 1, start1, rb1, hq); else p1_consume<0>(lds, ws, lane, w & 1, start1, rb1, hq);
        STAMP(1); STAMPM(1, 14); STAMPM(5, 2);
        LBAR();
        VMWAIT(0);
        PIN(e_raw);
        tag = ((u64)(unsigned)rfl((int)(e_raw >> 32)) << 32 | (unsigned)rfl((int)e_raw)) + 1;
        publish(fl + FL_1 + (hq * 32 + rb1) * 4 + w, tag, lane);
        STAMP(2); STAMPM(5, 3);
        LBAR();
        STAMP(3);
        q0 = lds128(lds + M_Q + qp * 2048 + l16); q1 = lds128(lds + M_Q + qp * 2048 + 1024 + l16);
        const char* dsrc = ksrc + (w >> 1) * (WS_V - WS_K) + (w & 1) * 4096;
        const unsigned ddst = lb + w * 32768;
        const unsigned swo = WS_WO + (cb * 2 + (w >> 1)) * 32768;
        GAS u64* frb = fl + FL_R + 4 * (224 + 4 * cb) + (lane & 15);
        u32x4 f1, fp;
        bool fpok;
        u64 fb;
        attn_main(lds, ka0, va0, vstep, q0, q1, o0, o1, [&](int c, int i) __attribute__((always_inline)) {
            const int n = c * 8 + i;
            if (n == 0) f1 = ld16(ws, l16, WS_FL + (FL_1 + (head >> 2) * 128) * 8);
            if (n >= 2 && n < 9) gld4(dsrc + ((qb + n - 1) & 7) * 8192, l16, ddst + (n - 1) * 4096);
            if (n >= 14 && n < 38 && (n & 1) == 0) {
                const int m = (n - 14) >> 1;
                if (m < 2) st16(ws, l16, WS_O + ((qb * 8 + 2 * w + m) * 32 + head) * 1024, poison);
                else if (m < 4) st16(ws, l16, WS_H + ((rb * 2 + (w & 1)) * 32 + cb * 4 + (w >> 1) * 2 + m - 2) * 1024, poison);
                else if (m == 4) st16(ws, r * 16, WS_HS + ((rb * 2 + (w & 1)) * 8 + cb) * 512, poison);
                else st16(ws, l16, WS_ACT + ((g4 * 4 + w) * 112 + j4 * 7 + m - 5) * 1024, poison);
            }
            if (n == 37) fb = __hip_atomic_load(frb, RLX);
            if (n >= 41 && n < 55) {
                stash[2 * (n - 41)] = ld16(ws, l16, swo + 2 * (n - 41) * 1024);
                stash[2 * (n - 41) + 1] = ld16(ws, l16, swo + (2 * (n - 41) + 1) * 1024);
            }
            if (n == 56 || n == 59) fp = ld16(ws, l16, WS_FL + FL_P * 8 + w * 2048 + (n == 59 ? 1024 : 0));
        }, [&](int c, int i, auto settle) __attribute__((always_inline)) {
            const int n = c * 8 + i;
            if (n == 2) STAMPM(4, 1); if (n == 6) STAMPM(4, 2); if (n == 7) STAMPM(4, 3); if (n == 14) STAMPM(4, 4); if (n == 15) STAMPM(4, 5); if (n == 20) STAMPM(4, 6); if (n == 21) STAMPM(4, 7);
            if (n == 24) STAMPM(4, 8); if (n == 32) STAMPM(4, 9); if (n == 36) STAMPM(4, 10); if (n == 40) STAMPM(4, 11); if (n == 48) STAMPM(4, 12); if (n == 56) STAMPM(4, 13); if (n == 60) STAMPM(4, 14);
            if (n == 2) {
                settle();
                PIN(f1);
                if (!__all(tags2(f1, tag))) { RCINC(0, 1); poll128(fl + FL_1 + (head >> 2) * 128, tag, lane); }
            } else if (n == 6) { VMWAIT(12); BAR(); }
            else if (n == 14) { VMWAIT(0); BAR(); }
            else if (n == 40) {
                settle();
                VMCLR();
                publish(fl + FL_P + b * 4 + w, tag, lane);
                if (!__all(fb == tag)) { RCINC(6, 1); poll(frb, tag); }
            } else if (n == 59) {
                PIN(fp);
                fpok = __all(tags2(fp, tag));
            } else if (n == 62) {
                settle();
                PIN(fp);
                if (!(fpok && __all(tags2(fp, tag)))) { RCINC(7, 1); poll128(fl + FL_P + w * 256, tag, lane); poll128(fl + FL_P + w * 256 + 128, tag, lane); }
            }
        });
        attn_finish(o0, o1, q0, q1, tag);
        {
        const int lane = fresh_lane(), r = lane & 31, h = lane >> 5;
        const int tt = w & 1, ct = w >> 1;
        const int ttile = rb * 2 + tt, ctile = cb * 2 + ct;
        u32x2 rx[4];
        {
            const char* p = x + (size_t)(ttile * 32 + r) * 1024 + (ctile * 32 + 4 * h) * 2;
#pragma unroll
            for (int g = 0; g < 4; ++g) rx[g] = *(const u32x2*)(p + g * 16);
        }
        u32x4 hr[2];
        {
            f32x16 acc = {};
            lptr ba = lds + opaque(S3 + tt * 8192 + l16);
#pragma unroll
            for (int m = 28; m < 32; ++m) stash[m] = ld16(ws, l16, swo + m * 1024);
            VMWAIT(8);
            publish(myf2, tag, lane);
            STAMPM(2, 4);
#pragma unroll
            for (int qd = 0; qd < 4; ++qd) {
                LBAR();
                STAMPM(2, 5 + qd);
                if (qd == 0) STAMP(8);
                u32x4 bf[8];
#pragma unroll
                for (int ks = 0; ks < 8; ++ks) bf[ks] = *(LAS u32x4*)(ba + qd * 16384 + ks * 1024);
                SBAR();
#pragma unroll
                for (int g = 0; g < 2; ++g) {
                    const unsigned oo = WS_O + (ttile * 32 + qd * 8 + g * 4) * 1024;
                    verify4(bf[4 * g], bf[4 * g + 1], bf[4 * g + 2], bf[4 * g + 3], ws, l16, oo, oo + 1024, oo + 2048, oo + 3072, fl + FL_2 + ttile * 32, 32, tag RC(2));
#pragma unroll
                    for (int ks = 4 * g; ks < 4 * g + 4; ++ks) acc = MFMA(BC8(stash[qd * 8 + ks]), BC8(bf[ks]), acc);
                }
            }
#pragma unroll
            for (int g = 0; g < 4; ++g) {
                hr[g >> 1][(g & 1) * 2] = pk(acc[4 * g] + bflo(rx[g][0]), acc[4 * g + 1] + bfhi(rx[g][0]));
                hr[g >> 1][(g & 1) * 2 + 1] = pk(acc[4 * g + 2] + bflo(rx[g][1]), acc[4 * g + 3] + bfhi(rx[g][1]));
            }
            st16(ws, l16, WS_H + (ttile * 32 + ctile * 2) * 1024, hr[0]);
            st16(ws, l16, WS_H + (ttile * 32 + ctile * 2 + 1) * 1024, hr[1]);
            float s1 = 0.f, s2 = 0.f;
            stat2(hr[0], s1, s2); stat2(hr[1], s1, s2);
            s1 = halfsum(s1); s2 = halfsum(s2);
            if (h == 0) __hip_atomic_store((GAS u64*)(ws_ + WS_HS) + ((ttile * 8 + cb) * 32 + r) * 2 + ct, (u64)__float_as_uint(s1) | ((u64)__float_as_uint(s2) << 32), RLX);
        }
        STAMPM(2, 9);
        LBAR();
        STAMP(9); STAMPM(2, 10);
        const int t4 = g4 * 4 + w;
        __builtin_amdgcn_s_sleep(SL3);
        u32x4 hs[4], hf[32];
        constexpr int HAHEAD = 12;
        const unsigned ho = WS_H + t4 * 32768;
#pragma unroll
        for (int i = 0; i < 4; ++i) hs[i] = ld16(ws, l16 + i * 1024, WS_HS + t4 * 4096);
#pragma unroll
        for (int ks = 0; ks < HAHEAD; ++ks) hf[ks] = ld16(ws, l16, ho + ks * 1024);
        STAMPM(2, 11);
        VMWAIT(4 + HAHEAD);
        STAMPM(2, 12);
        publish(fl + FL_3 + ttile * 16 + ctile, tag, lane);
        LBAR();
        STAMPM(2, 13);
        { const unsigned so = WS_HS + t4 * 4096; verify4(hs[0], hs[1], hs[2], hs[3], ws, l16, so, so + 1024, so + 2048, so + 3072, fl + FL_3 + t4 * 16, 16, tag RC(3)); }
        STAMP(10); STAMPM(2, 14);
        float s1 = 0.f, s2 = 0.f;
#pragma unroll
        for (int i = 0; i < 4; ++i) { s1 += __uint_as_float(hs[i][0]) + __uint_as_float(hs[i][2]); s2 += __uint_as_float(hs[i][1]) + __uint_as_float(hs[i][3]); }
        s1 = halfsum(s1); s2 = halfsum(s2);
        const float m = s1 * (1.f / D);
        const float vv = fmaxf(s2 * (1.f / D) - m * m, 0.f) + 1e-5f;
        const float rs = __builtin_amdgcn_rsqf(vv);
        constexpr float C1 = -2.f * 0.7978845608028654f * 1.4426950408889634f, C2 = C1 * 0.044715f;
        const float k1 = C1 * rs, k2 = C2 * rs * rs * rs;
        SBAR();
        f32x16 A[4];
        lptr wlo = lds + opaque(l16), whi = lds + opaque(S2 + l16);
        {
            bf16x8 wf[2][3];
            wf[0][0] = lds128(wlo); wf[0][1] = lds128(wlo + S1); wf[0][2] = lds128(whi);
            SBAR();
#pragma unroll
            for (int ks = 0; ks < 32; ++ks) {
                const int c = ks & 1, n = c ^ 1;
                if ((ks & 3) == 0) verify4(hf[ks], hf[ks + 1], hf[ks + 2], hf[ks + 3], ws, l16, ho + ks * 1024, ho + (ks + 1) * 1024, ho + (ks + 2) * 1024, ho + (ks + 3) * 1024, fl + FL_3 + t4 * 16, 16, tag RC(4));
                if (ks + HAHEAD < 32) hf[ks + HAHEAD] = ld16(ws, l16, ho + (ks + HAHEAD) * 1024);
                if (ks < 31) wf[n][0] = lds128(wlo + (ks + 1) * 1024);
                SBAR(); A[0] = MFMA(wf[c][0], BC8(hf[ks]), ks ? A[0] : f32x16{}); SBAR();
                if (ks < 31) wf[n][1] = lds128(wlo + S1 + (ks + 1) * 1024);
                SBAR(); A[1] = MFMA(wf[c][1], BC8(hf[ks]), ks ? A[1] : f32x16{}); SBAR();
                if (ks < 31) wf[n][2] = lds128(whi + (ks + 1) * 1024);
                SBAR(); A[2] = MFMA(wf[c][2], BC8(hf[ks]), ks ? A[2] : f32x16{}); SBAR();
            }
        }
        LAS float* cs = (LAS float*)(lds + opaque(M0 + 16 * h));
        const unsigned adst = WS_ACT + (t4 * 112 + j4 * 7) * 1024;
        float ea[4], eu[4], et[4];
        f32x4 cg, cu;
        u32x4 eo;
        auto epi = [&](auto G_) __attribute__((always_inline)) {
            constexpr int G = decltype(G_)::v;
            constexpr int hf_ = G / 13, st = G % 13, t = hf_ >> 1, e0 = (hf_ & 1) * 4;

            if constexpr (st == 0) {
#pragma unroll
                for (int e = 0; e < 4; ++e) { ea[e] = fmaf(-m, cg[e], A[t & 3][e0 + e]); PIN(ea[e]); }
            } else if constexpr (st == 1) {
#pragma unroll
                for (int e = 0; e < 4; ++e) { eu[e] = fmaf(-m, cu[e], A[t & 3][8 + e0 + e]); PIN(eu[e]); }
            } else if constexpr (st == 2) {
#pragma unroll
                for (int e = 0; e < 4; ++e) { et[e] = ea[e] * ea[e]; PIN(et[e]); }
            } else if constexpr (st == 3) {
#pragma unroll
                for (int e = 0; e < 4; ++e) { et[e] = fmaf(k2, et[e], k1); PIN(et[e]); }
            } else if constexpr (st == 4) {
#pragma unroll
                for (int e = 0; e < 4; ++e) { et[e] *= ea[e]; PIN(et[e]); }
            } else if constexpr (st == 5) {
#pragma unroll
                for (int e = 0; e < 4; ++e) { eu[e] *= ea[e]; PIN(eu[e]); }
            } else if constexpr (st == 6 || st == 7) {
#pragma unroll
                for (int e = 2 * (st - 6); e < 2 * (st - 6) + 2; ++e) { et[e] = __builtin_amdgcn_exp2f(et[e]); PIN(et[e]); }
                if constexpr (hf_ + 1 < 14) {
                    LAS float* c = cs + ((hf_ + 1) >> 1) * 32 + ((hf_ + 1) & 1) * 8;
                    if (st == 6) cg = *(LAS f32x4*)c; else cu = *(LAS f32x4*)(c + 16);
                }
            } else if constexpr (st == 8) {
#pragma unroll
                for (int e = 0; e < 4; ++e) { et[e] = fmaf(et[e], vv, vv); PIN(et[e]); }
            } else if constexpr (st == 9 || st == 10) {
#pragma unroll
                for (int e = 2 * (st - 9); e < 2 * (st - 9) + 2; ++e) { et[e] = __builtin_amdgcn_rcpf(et[e]); PIN(et[e]); }
            } else if constexpr (st == 11) {
#pragma unroll
                for (int e = 0; e < 4; ++e) { eu[e] *= et[e]; PIN(eu[e]); }
            } else {
                eo[e0 >> 1] = pk(eu[0], eu[1]); eo[(e0 >> 1) + 1] = pk(eu[2], eu[3]);
                if (e0) st16(ws, l16, adst + t * 1024, eo); else { PIN(eo[0]); PIN(eo[1]); }
            }
        };
        auto ftile = [&](auto t_, lptr p) __attribute__((always_inline)) {
            constexpr int t = decltype(t_)::v;
            bf16x8 wf[4];
            wf[0] = lds128(p); wf[1] = lds128(p + 1024); wf[2] = lds128(p + 2048);
            SBAR();
            sfor<0, 32>([&](auto ks_) __attribute__((always_inline)) {
                constexpr int ks = decltype(ks_)::v;
                if (ks + 3 < 32) wf[(ks + 3) & 3] = lds128(p + (ks + 3) * 1024);
                epi(IC<(t - 3) * 32 + ks>{});
                SBAR();
                A[t & 3] = MFMA(wf[ks & 3], BC8(hf[ks]), ks ? A[t & 3] : f32x16{});
                SBAR();
            });
        };
        STAMPM(2, 15);
        LBAR();
        cg = *(LAS f32x4*)cs; cu = *(LAS f32x4*)(cs + 16);
        STAMP(11); STAMPM(3, 1);
        ftile(IC<3>{}, whi + 32768);
        STAMPM(3, 2);
        LBAR();
        STAMPM(3, 3);
        ftile(IC<4>{}, wlo);
        STAMPM(3, 4);
        LBAR();
        STAMPM(3, 5);
        ftile(IC<5>{}, wlo + S1);
        STAMPM(3, 6);
        LBAR();
        STAMPM(3, 7);
        ftile(IC<6>{}, whi);
        STAMPM(3, 8);
        asm volatile("s_waitcnt lgkmcnt(0)" ::: "memory");
        if (lane == 0) __hip_atomic_fetch_add((LAS unsigned*)(lds + M_CNT), 1u, __ATOMIC_RELAXED, __HIP_MEMORY_SCOPE_WORKGROUP);
        sfor<128, 14 * 13>(epi);
        STAMPM(3, 9);
        STAMP(12);
        f32x16 C[4];
        {
            u32x4 af[8][2];
            auto aoff = [&](int k, int t) { return WS_ACT + ((rb * 2 + t) * 112 + (4 * w + (k & 3)) * 7 + 5 + (k >> 2)) * 1024; };
#pragma unroll
            for (int k = 0; k < 8; ++k) { af[k][0] = ld16(ws, l16, aoff(k, 0)); af[k][1] = ld16(ws, l16, aoff(k, 1)); }
            lptr wa0 = lds + opaque(w * 4096 + l16), wa3 = lds + opaque(S3 + w * 4096 + l16);
            bf16x8 wf[8][2];
#pragma unroll
            for (int k = 0; k < 8; ++k) { lptr p = (k < 4 ? wa3 : wa0) + (k & 3) * 1024; wf[k][0] = lds128(p); wf[k][1] = lds128(p + 16384); }
            STAMPM(3, 10);
            VMWAIT(16);
            STAMPM(3, 11);
            publish(fl + FL_4 + t4 * 16 + j4, tag, lane);
            STAMP(13); STAMPM(3, 12);
#pragma unroll
            for (int k = 0; k < 8; ++k) {
                if ((k & 1) == 0) verify4(af[k][0], af[k][1], af[k + 1][0], af[k + 1][1], ws, l16, aoff(k, 0), aoff(k, 1), aoff(k + 1, 0), aoff(k + 1, 1), fl + FL_4 + rb * 32, 32, tag RC(5));
#pragma unroll
                for (int q = 0; q < 4; ++q) C[q] = MFMA(wf[k][q >> 1], BC8(af[k][q & 1]), k ? C[q] : f32x16{});
                SBAR();
            }
            STAMPM(3, 13);
#pragma unroll
            for (int q = 0; q < 4; ++q) {
                if (q != w) {
                    const int pc = (q - w - 1) & 3;
                    lptr p = pc == 0 ? wa3 : (pc == 1 ? wa3 + 16384 : wa0);
#pragma unroll
                    for (int g = 0; g < 4; ++g) *(LAS f32x4*)(p + g * 1024) = (f32x4){C[q][4 * g], C[q][4 * g + 1], C[q][4 * g + 2], C[q][4 * g + 3]};
                }
            }
        }
        f32x16 acc = w == 0 ? C[0] : (w == 1 ? C[1] : (w == 2 ? C[2] : C[3]));
        STAMPM(3, 14);
        LBAR();
        STAMPM(3, 15);
        {
            lptr lp1 = lds + opaque(S1 + w * 4096 + l16);
#pragma unroll
            for (int s = 0; s < 7; ++s) {
                lptr p;
                if (s < 4) p = lp1 + s * 16384;
                else { const int pc = s - 4, src_w = (w - pc - 1) & 3; p = lds + opaque(src_w * 4096 + l16 + (pc == 0 ? S3 : (pc == 1 ? S3 + 16384 : 0))); }
#pragma unroll
                for (int g = 0; g < 4; ++g) {
                    const f32x4 v = *(LAS f32x4*)(p + g * 1024);
#pragma unroll
                    for (int j = 0; j < 4; ++j) acc[4 * g + j] += v[j];
                }
            }
        }
        STAMP(14);
        char* p = out + (size_t)(ttile * 32 + r) * 1024 + (ctile * 32 + 4 * h) * 2;
#pragma unroll
        for (int g = 0; g < 4; ++g) {
            const unsigned a = hr[g >> 1][(g & 1) * 2], c = hr[g >> 1][(g & 1) * 2 + 1];
            *(u32x2*)(p + g * 16) = (u32x2){pk(acc[4 * g] + bflo(a), acc[4 * g + 1] + bfhi(a)), pk(acc[4 * g + 2] + bflo(c), acc[4 * g + 3] + bfhi(c))};
        }
        asm volatile(".Lpf_end1:");
        }
    }
    STAMP(15); STAMP_FLUSH(ws_);
}

extern "C" void kernel_launch(void* const* d_in, const int* in_sizes, int n_in, void* d_out, int out_size, void* d_ws, size_t ws_size,
                              hipStream_t stream) {
    (void)hipFuncSetAttribute((const void*)fused, hipFuncAttributeMaxDynamicSharedMemorySize, LDS_BYTES);
    fused<<<256, 512, LDS_BYTES, stream>>>((const char*)d_in[0], (const char*)d_in[1], (const char*)d_in[2], (const char*)d_in[4],
                                           (const char*)d_in[5], (const char*)d_in[6], (char*)d_out, (char*)d_ws);
}
```

```cpp
#include <hip/hip_runtime.h>
#include <hip/hip_bf16.h>
#include <stdint.h>

typedef __attribute__((ext_vector_type(8))) short bf16x8;
typedef __attribute__((ext_vector_type(4))) short s16x4;
typedef __attribute__((ext_vector_type(16))) float f32x16;
typedef __attribute__((ext_vector_type(4))) float f32x4;
typedef __attribute__((ext_vector_type(2))) float f32x2;
typedef __attribute__((ext_vector_type(4))) unsigned u32x4;
typedef __attribute__((ext_vector_type(2))) unsigned u32x2;
typedef __attribute__((ext_vector_type(2))) __bf16 bf16x2;
typedef unsigned long long u64;
#define LAS __attribute__((address_space(3)))
#define GAS __attribute__((address_space(1)))
typedef LAS char* lptr;

#define MFMA(a, b, c) __builtin_amdgcn_mfma_f32_32x32x16_bf16(a, b, c, 0, 0, 0)
#define SBAR() __builtin_amdgcn_sched_barrier(0)
#define WAITBAR(N) asm volatile("s_waitcnt vmcnt(%0) lgkmcnt(0)\n\ts_barrier" :: "i"(N) : "memory")
#define LBAR() asm volatile("s_waitcnt lgkmcnt(0)\n\ts_barrier" ::: "memory")
#define VMWAIT(N) asm volatile("s_waitcnt vmcnt(%0)" :: "i"(N) : "memory")
#define VMCLR() __builtin_amdgcn_s_waitcnt(0x0F70)
#define RLX __ATOMIC_RELAXED, __HIP_MEMORY_SCOPE_AGENT

#define STAMP_DECL do {} while (0)
#define RCARG
#define RC(i)
#define RCADD(n) do {} while (0)
#define RCINC(i, n) do {} while (0)
#define STAMP(idx) do {} while (0)
#define STAMPM(m, idx) do {} while (0)
#define STAMP_FLUSH(ws) do {} while (0)

constexpr int D = 512;
constexpr unsigned WS_Q = 0;
constexpr unsigned WS_K = WS_Q + (2u << 20);
constexpr unsigned WS_V = WS_K + (2u << 20);
constexpr unsigned WS_O = WS_V + (2u << 20);
constexpr unsigned WS_H = WS_O + (2u << 20);
constexpr unsigned WS_ACT = WS_H + (2u << 20);
constexpr unsigned WS_WO = WS_ACT + (7u << 20);
constexpr unsigned WS_WGU = WS_WO + (512u << 10);
constexpr unsigned WS_WD = WS_WGU + (3584u << 10);
constexpr unsigned WS_HS = WS_WD + (1792u << 10);
constexpr unsigned WS_CS = WS_HS + (256u << 10);
constexpr unsigned WS_FL = WS_CS + (256u << 10);
constexpr unsigned WS_END = WS_FL + 65536;
constexpr int FL_P = 0, FL_1 = 1024, FL_R = 2048, FL_2 = 3072, FL_3 = 5120, FL_4 = 6144, FL_E = 7168;
constexpr unsigned POISON = 0xFFFFFFFFu;
constexpr int S0 = 0, S1 = 32768, S2 = 65536, S3 = 98304, M0 = 131072, LDS_BYTES = 163840;
constexpr int M_Q = M0, M_X = M0, M_STG = M0 + 18432, M_CONST = M0 + 26624, M_WORD = M0 + 30720, M_CNT = M0 + 16384;
constexpr int P1_W = 65536, P1_SLOT = 24576;

__device__ __forceinline__ void glds3(const void* sbase, unsigned voff, unsigned ldsaddr) {
    asm volatile("s_mov_b32 m0, %2\n\ts_nop 0\n\tglobal_load_lds_dwordx4 %0, %1\n\tglobal_load_lds_dwordx4 %0, %1 offset:1024\n\tglobal_load_lds_dwordx4 %0, %1 offset:2048"
                 :: "v"(voff), "s"(sbase), "s"(ldsaddr) : "memory");
}
__device__ __forceinline__ void glds3_upper(int w, const void* sbase, unsigned voff, unsigned ldsaddr) {
    asm volatile("s_cmp_lt_u32 %3, 4\n\ts_cbranch_scc1 1f\n\ts_mov_b32 m0, %2\n\ts_nop 0\n\tglobal_load_lds_dwordx4 %0, %1\n\t"
                 "global_load_lds_dwordx4 %0, %1 offset:1024\n\tglobal_load_lds_dwordx4 %0, %1 offset:2048\n1:"
                 :: "v"(voff), "s"(sbase), "s"(ldsaddr), "s"(w) : "scc", "memory");
}
__device__ __forceinline__ void touch(unsigned& d, const char* p, int ix, int first, int n) {
    asm volatile("s_cmp_lt_u32 %2, %3\n\ts_cbranch_scc0 1f\n\ts_load_dword %0, %1, 0\n1:" : "+s"(d) : "s"(p), "s"(ix - first), "s"(n) : "scc", "memory");
}
__device__ __forceinline__ void gld1(const void* sbase, unsigned voff, unsigned ldsaddr) {
    asm volatile("s_mov_b32 m0, %2\n\ts_nop 0\n\tglobal_load_lds_dwordx4 %0, %1 sc1" :: "v"(voff), "s"(sbase), "s"(ldsaddr) : "memory");
}
__device__ __forceinline__ void gld2(const void* sbase, unsigned voff, unsigned ldsaddr) {
    asm volatile("s_mov_b32 m0, %2\n\ts_nop 0\n\tglobal_load_lds_dwordx4 %0, %1 sc1\n\tglobal_load_lds_dwordx4 %0, %1 offset:1024 sc1"
                 :: "v"(voff), "s"(sbase), "s"(ldsaddr) : "memory");
}
__device__ __forceinline__ void gld4(const void* sbase, unsigned voff, unsigned ldsaddr) {
    asm volatile("s_mov_b32 m0, %2\n\ts_nop 0\n\tglobal_load_lds_dwordx4 %0, %1 sc1\n\tglobal_load_lds_dwordx4 %0, %1 offset:1024 sc1\n\t"
                 "global_load_lds_dwordx4 %0, %1 offset:2048 sc1\n\tglobal_load_lds_dwordx4 %0, %1 offset:3072 sc1"
                 :: "v"(voff), "s"(sbase), "s"(ldsaddr) : "memory");
}
__device__ __forceinline__ void gld8(const char* sbase, unsigned voff, unsigned ldsaddr) {
    asm volatile("s_mov_b32 m0, %2\n\ts_nop 0\n\tglobal_load_lds_dwordx4 %0, %1 offset:-4096 sc1\n\tglobal_load_lds_dwordx4 %0, %1 offset:-3072 sc1\n\t"
                 "global_load_lds_dwordx4 %0, %1 offset:-2048 sc1\n\tglobal_load_lds_dwordx4 %0, %1 offset:-1024 sc1\n\t"
                 "global_load_lds_dwordx4 %0, %1 sc1\n\tglobal_load_lds_dwordx4 %0, %1 offset:1024 sc1\n\t"
                 "global_load_lds_dwordx4 %0, %1 offset:2048 sc1\n\tglobal_load_lds_dwordx4 %0, %1 offset:3072 sc1"
                 :: "v"(voff), "s"(sbase + 4096), "s"(ldsaddr + 4096) : "memory");
}
__device__ __forceinline__ bf16x8 lds128(lptr p) { return *(const LAS bf16x8*)p; }
__device__ __forceinline__ s16x4 ldstr(lptr p) { return __builtin_amdgcn_ds_read_tr16_b64_v4i16((LAS s16x4*)p); }
__device__ __forceinline__ bf16x8 cat(s16x4 a, s16x4 b) { return __builtin_shufflevector(a, b, 0, 1, 2, 3, 4, 5, 6, 7); }
__device__ __forceinline__ unsigned pk(float lo, float hi) { bf16x2 r = {(__bf16)lo, (__bf16)hi}; return __builtin_bit_cast(unsigned, r); }
__device__ __forceinline__ float dot2(unsigned a, unsigned b, float c) {
    return __builtin_amdgcn_fdot2_f32_bf16(__builtin_bit_cast(bf16x2, a), __builtin_bit_cast(bf16x2, b), c, false);
}
__device__ __forceinline__ float bflo(unsigned u) { return __uint_as_float(u << 16); }
__device__ __forceinline__ float bfhi(unsigned u) { return __uint_as_float(u & 0xffff0000u); }
constexpr unsigned ONES2 = 0x3F803F80u;
__device__ __forceinline__ void stat2(u32x4 u, float& s1, float& s2) {
#pragma unroll
    for (int i = 0; i < 4; ++i) { s1 = dot2(u[i], ONES2, s1); s2 = dot2(u[i], u[i], s2); }
}
__device__ __forceinline__ void stat1(bf16x8 f, float& s1) {
    u32x4 u = __builtin_bit_cast(u32x4, f);
#pragma unroll
    for (int i = 0; i < 4; ++i) s1 = dot2(u[i], ONES2, s1);
}
__device__ __forceinline__ unsigned opaque(unsigned x) { asm volatile("" : "+v"(x)); return x; }
template <int I> struct IC { static constexpr int v = I; };
template <int B, int E, class Fn> __device__ __forceinline__ void sfor(Fn&& f) { if constexpr (B < E) { f(IC<B>{}); sfor<B + 1, E>(f); } }
#define PIN(x) asm volatile("" : "+v"(x))
__device__ __forceinline__ int fresh_lane() {
    int x; asm volatile("v_mbcnt_lo_u32_b32 %0, -1, 0\n\tv_mbcnt_hi_u32_b32 %0, -1, %0" : "=v"(x)); return x;
}
__device__ __forceinline__ int rfl(int x) { return __builtin_amdgcn_readfirstlane(x); }
__device__ __forceinline__ float halfsum(float x) {
    auto r = __builtin_amdgcn_permlane32_swap(__float_as_uint(x), __float_as_uint(x), false, false);
    return __uint_as_float(r[0]) + __uint_as_float(r[1]);
}
typedef __amdgpu_buffer_rsrc_t rsrc_t;
__device__ __forceinline__ void st16(rsrc_t r, unsigned voff, unsigned soff, u32x4 v) {
    __builtin_amdgcn_raw_buffer_store_b128(v, r, voff, soff, 16);
    asm volatile("s_nop 1" :: "v"(v));
}
__device__ __forceinline__ u32x4 ld16(rsrc_t r, unsigned voff, unsigned soff) { return __builtin_amdgcn_raw_buffer_load_b128(r, voff, soff, 16); }
__device__ __forceinline__ void publish(GAS u64* f, u64 tag, int lane) { if (lane == 0) __hip_atomic_store(f, tag, RLX); }
__device__ __forceinline__ void poll(GAS u64* f, u64 tag) {
#pragma nounroll
    for (unsigned spins = 0; spins < (1u << 16); ++spins) {
        const u64 v = __hip_atomic_load(f, RLX);
        if (__all(v == tag)) break;
        __builtin_amdgcn_s_sleep(2);
    }
    asm volatile("" ::: "memory");
}
__device__ __forceinline__ void poll(GAS u64* f, int n, u64 tag, int lane) { poll(f + (lane & (n - 1)), tag); }
#define SL1 20
#define SL2 32
#define OUT_SC1 0
#define COLD_BYTES 9728
#define SL4 16
#define SL3 24
__device__ __forceinline__ unsigned mx(u32x4 v, unsigned m) { return max(max(v[2], v[3]), max(max(v[0], v[1]), m)); }
__device__ __forceinline__ void verify4(u32x4& a, u32x4& b, u32x4& c, u32x4& d, rsrc_t ws, unsigned voff, unsigned oa, unsigned ob, unsigned oc,
                                        unsigned od, GAS u64* flags, int n, u64 tag RCARG) {
    if (__builtin_expect(__any(mx(d, mx(c, mx(b, mx(a, 0u)))) == POISON), 0)) {
#pragma nounroll
        for (int t = 0;; ++t) {
            if (t == 32) poll(flags, n, tag, (int)(voff >> 4)); else __builtin_amdgcn_s_sleep(2);
            a = ld16(ws, voff, oa); b = ld16(ws, voff, ob); c = ld16(ws, voff, oc); d = ld16(ws, voff, od);
            VMCLR();
            RCADD(1);
            if (t == 32 || !__any(mx(d, mx(c, mx(b, mx(a, 0u)))) == POISON)) break;
        }
    }
}
__device__ __forceinline__ void arrive(unsigned ldsaddr) { asm volatile("ds_add_u32 %0, %1" :: "v"(ldsaddr), "v"(1u) : "memory"); }
__device__ __forceinline__ void wait_lds(lptr p, unsigned n) {
#pragma nounroll
    for (int t = 0; t < (1 << 20); ++t) {
        if ((unsigned)rfl(__hip_atomic_load((LAS unsigned*)p, __ATOMIC_RELAXED, __HIP_MEMORY_SCOPE_WORKGROUP)) >= n) break;
        __builtin_amdgcn_s_sleep(1);
    }
    asm volatile("" ::: "memory");
}
__device__ __forceinline__ void poll128(GAS u64* f, u64 tag, int lane) { poll(f + lane, tag); poll(f + 64 + lane, tag); }
__device__ __forceinline__ bool tags2(u32x4 v, u64 tag) { return v[0] == (unsigned)tag && v[2] == (unsigned)tag && v[1] == (unsigned)(tag >> 32) && v[3] == (unsigned)(tag >> 32); }
#define BC8(x) __builtin_bit_cast(bf16x8, x)
#define BAR() asm volatile("s_barrier" ::: "memory")

__device__ __forceinline__ float sum8(float x) {
    x += __builtin_bit_cast(float, __builtin_amdgcn_mov_dpp(__builtin_bit_cast(int, x), 0xB1, 0xF, 0xF, true));
    x += __builtin_bit_cast(float, __builtin_amdgcn_mov_dpp(__builtin_bit_cast(int, x), 0x4E, 0xF, 0xF, true));
    x += __builtin_bit_cast(float, __builtin_amdgcn_mov_dpp(__builtin_bit_cast(int, x), 0x141, 0xF, 0xF, true));
    return x;
}
#define XWAIT(A, B, C, D, NL, NC) asm volatile("s_cmp_lt_u32 %4, 4\n\ts_cbranch_scc1 1f\n\ts_waitcnt vmcnt(" #NL ")\n\ts_branch 2f\n1:\n\ts_waitcnt vmcnt(" #NC ")\n2:" \
                                               : "+v"(A), "+v"(B), "+v"(C), "+v"(D) : "s"(w) : "scc", "memory")
template <class Side>
__device__ __forceinline__ void p1_norm(lptr lds, const char* xs, int lane, int w, int rot, Side side) {
    const int pos = lane & 7, row = 8 * w + (lane >> 3);
    const unsigned xo = row * 1024 + pos * 16;
    u32x4 xr[8];
#pragma unroll
    for (int c = 0; c < 8; ++c) {
        asm volatile("global_load_dwordx4 %0, %1, %2" : "=v"(xr[c]) : "v"(xo), "s"(xs + ((c + rot) & 7) * 128) : "memory");
        if (c == 3) asm volatile("s_barrier" ::: "memory");
    }
    side(-1);
    float s1 = 0.f, s2 = 0.f, t1 = 0.f, t2 = 0.f;
    XWAIT(xr[0], xr[1], xr[2], xr[3], 10, 4);
    stat2(xr[0], s1, s2); stat2(xr[1], t1, t2); stat2(xr[2], s1, s2); stat2(xr[3], t1, t2);
    PIN(s1); PIN(s2); PIN(t1); PIN(t2);
    XWAIT(xr[4], xr[5], xr[6], xr[7], 6, 0);
    stat2(xr[4], s1, s2); stat2(xr[5], t1, t2); stat2(xr[6], s1, s2); stat2(xr[7], t1, t2);
    s1 = sum8(s1 + t1); s2 = sum8(s2 + t2);
    const float m = s1 * (1.f / D);
    const float rs = __builtin_amdgcn_rsqf(fmaxf(s2 * (1.f / D) - m * m, 0.f) + 1e-5f), nb = -m * rs;
    const unsigned a = row * 128 + ((pos ^ ((row >> 1) & 7)) << 4);
#pragma unroll
    for (int c = 0; c < 8; ++c) {
        float lo[4], hi[4];
#pragma unroll
        for (int i = 0; i < 4; ++i) { lo[i] = bflo(xr[c][i]); hi[i] = bfhi(xr[c][i]); }
        asm volatile("" : "+v"(lo[0]), "+v"(lo[1]), "+v"(lo[2]), "+v"(lo[3]), "+v"(hi[0]), "+v"(hi[1]), "+v"(hi[2]), "+v"(hi[3]));
#pragma unroll
        for (int i = 0; i < 4; ++i) { lo[i] = fmaf(lo[i], rs, nb); hi[i] = fmaf(hi[i], rs, nb); }
        asm volatile("" : "+v"(lo[0]), "+v"(lo[1]), "+v"(lo[2]), "+v"(lo[3]), "+v"(hi[0]), "+v"(hi[1]), "+v"(hi[2]), "+v"(hi[3]));
        *(LAS u32x4*)(lds + a + ((c + rot) & 7) * 8192) = (u32x4){pk(lo[0], hi[0]), pk(lo[1], hi[1]), pk(lo[2], hi[2]), pk(lo[3], hi[3])};
        side(c);
    }
}
__device__ __forceinline__ void p1_store(rsrc_t ws, int lane, int u, int e, int tile, int hq, u32x4 v) {
    const int head0 = hq * 4 + (u & 1) * 2;
    if (u < 4) st16(ws, lane * 16, (u < 2 ? WS_Q : WS_K) + ((head0 + e) * 64 + tile) * 1024, v);
    else st16(ws, ((lane >> 4) & 1) * 65536 + (lane >> 5) * 256 + (lane & 15) * 16, WS_V + (head0 * 64 + tile) * 1024 + e * 512, v);
}
template <int CG>
__device__ __forceinline__ void p1_consume(lptr lds, rsrc_t ws, int lane, int t, int start, int rb, int hq, unsigned xa, const unsigned (&wa)[2], f32x16 (&acc)[3]) {
    bf16x8 xf[4], wf[4][3];
    auto rdx = [&](int slot, unsigned a) { xf[slot] = lds128(lds + a); };
    auto rdw = [&](int slot, int i, lptr p0, lptr p1, int ks) {
        const int u = CG * 3 + i;
        lptr a = ((u & 1) ? p1 : p0) + (u >> 1) * 1024 + ks * 6144;
        wf[slot][i] = cat(ldstr(a), ldstr(a + 512));
    };
    unsigned xc = (start & 7) * 8192;
    lptr c0 = lds + opaque(wa[0]), c1 = lds + opaque(wa[1]);
#pragma unroll
    for (int ks = 0; ks < 2; ++ks) { rdx(ks, opaque(xc + (xa ^ (ks << 5)))); rdw(ks, 0, c0, c1, ks); rdw(ks, 1, c0, c1, ks); rdw(ks, 2, c0, c1, ks); }
    SBAR();
#pragma unroll 1
    for (int ch = 0; ch < 8; ++ch) {
        const unsigned xn = ((ch + 1 + start) & 7) * 8192, sn = ((ch + 1) & 3) * P1_SLOT;
        lptr n0 = lds + opaque(sn + wa[0]), n1 = lds + opaque(sn + wa[1]);
        const unsigned xa2 = opaque(xc + (xa ^ 64)), xa3 = opaque(xc + (xa ^ 96)), xb0 = opaque(xn + xa), xb1 = opaque(xn + (xa ^ 32));
#pragma unroll
        for (int ks = 0; ks < 4; ++ks) {
            const int s = (ks + 2) & 3;
            if (ks == 2 && ch < 7) LBAR();
#pragma unroll
            for (int i = 0; i < 3; ++i) {
                if (i == 0) rdx(s, ks == 0 ? xa2 : (ks == 1 ? xa3 : (ks == 2 ? xb0 : xb1)));
                if (ks < 2) rdw(s, i, c0, c1, ks + 2); else rdw(s, i, n0, n1, ks - 2);
                SBAR();
                acc[i] = (CG * 3 + i >= 4) ? MFMA(xf[ks], wf[ks][i], acc[i]) : MFMA(wf[ks][i], xf[ks], acc[i]);
                SBAR();
            }
        }
        xc = xn; c0 = n0; c1 = n1;
    }
#pragma unroll
    for (int i = 0; i < 3; ++i) {
        const float sc = (CG * 3 + i < 2) ? 0.25f * 1.4426950408889634f : 1.f;
#pragma unroll
        for (int e = 0; e < 2; ++e) {
            u32x4 v;
#pragma unroll
            for (int j = 0; j < 4; ++j) v[j] = pk(acc[i][8 * e + 2 * j] * sc, acc[i][8 * e + 2 * j + 1] * sc);
            p1_store(ws, lane, CG * 3 + i, e, rb * 2 + t, hq, v);
        }
    }
}

#define A_EXP(d, s) asm volatile("v_exp_f32 %0, %1" : "=v"(d) : "v"(s))
#define A_CVT(d, a, b) asm volatile("v_cvt_pk_bf16_f32 %0, %1, %2" : "=v"(d) : "v"(a), "v"(b))
#define A_QK(d, k, q) asm volatile("v_mfma_f32_32x32x16_bf16 %0, %1, %2, 0" : "=&v"(d) : "v"(k), "v"(q))
#define A_PV(o, v, p) asm volatile("v_mfma_f32_32x32x16_bf16 %0, %1, %2, %0" : "+v"(o) : "v"(v), "v"(p))
#define UNIT(SC, SN, KN, QN, OPREV, VPREV1, OC, VC0, LOADS) do { \
        float e0, e1, e2, e3, e4, e5, e6, e7; u32x4 p0; \
        A_QK(SN, KN, QN); \
        A_EXP(e0, SC[0]); A_EXP(e1, SC[1]); \
        A_PV(OPREV, VPREV1, p1); \
        LOADS; SBAR(); \
        A_EXP(e2, SC[2]); A_EXP(e3, SC[3]); A_CVT(p0[0], e0, e1); \
        A_EXP(e4, SC[4]); A_EXP(e5, SC[5]); A_CVT(p0[1], e2, e3); \
        A_EXP(e6, SC[6]); A_EXP(e7, SC[7]); A_CVT(p0[2], e4, e5); \
        A_EXP(e0, SC[8]); A_CVT(p0[3], e6, e7); A_EXP(e1, SC[9]); \
        A_EXP(e2, SC[10]); A_PV(OC, VC0, p0); A_EXP(e3, SC[11]); A_CVT(p1[0], e0, e1); \
        A_EXP(e4, SC[12]); A_EXP(e5, SC[13]); A_CVT(p1[1], e2, e3); \
        A_EXP(e6, SC[14]); A_EXP(e7, SC[15]); A_CVT(p1[2], e4, e5); \
        asm volatile("s_nop 0"); A_CVT(p1[3], e6, e7); \
    } while (0)

template <class Hook, class Gap>
__device__ __forceinline__ void attn_main(lptr lds, unsigned ka, unsigned va, unsigned vstep, bf16x8 q0, bf16x8 q1, f32x16& o0, f32x16& o1, Hook hook, Gap gap) {
    f32x16 sa, sb;
    u32x4 p1 = {0u, 0u, 0u, 0u};
    bf16x8 kc, kn, va0, va1, vb0, vb1;
    kc = lds128(lds + ka); va0 = lds128(lds + va); va1 = lds128(lds + va + 512);
    vb1 = va1;
    A_QK(sa, kc, q0);
    asm volatile("s_nop 7\n\ts_nop 7" : "+v"(sa), "+v"(p1), "+v"(o0), "+v"(o1));
    auto settle_a = [&]() { asm volatile("s_nop 3" : "+v"(o0), "+v"(o1), "+v"(sa)); };
    auto settle_b = [&]() { asm volatile("s_nop 3" : "+v"(o0), "+v"(o1), "+v"(sb)); };
#pragma unroll
    for (int c = 0; c < 8; ++c) {
        __builtin_amdgcn_s_setprio(3);
        gap(c, 0, settle_a);
        UNIT(sa, sb, kc, q1, o1, vb1, o0, va0, (kn = lds128(lds + ka + 1024), hook(c, 0)));
        gap(c, 1, settle_b);
        UNIT(sb, sa, kn, q0, o0, va1, o1, va0, (vb0 = lds128(lds + va + 1024), vb1 = lds128(lds + va + 1536), hook(c, 1)));
        __builtin_amdgcn_s_setprio(2);
        gap(c, 2, settle_a);
        UNIT(sa, sb, kn, q1, o1, va1, o0, vb0, (kc = lds128(lds + ka + 2048), hook(c, 2)));
        gap(c, 3, settle_b);
        UNIT(sb, sa, kc, q0, o0, vb1, o1, vb0, (va0 = lds128(lds + va + 2048), va1 = lds128(lds + va + 2560), hook(c, 3)));
        __builtin_amdgcn_s_setprio(1);
        gap(c, 4, settle_a);
        UNIT(sa, sb, kc, q1, o1, vb1, o0, va0, (kn = lds128(lds + ka + 3072), hook(c, 4)));
        gap(c, 5, settle_b);
        UNIT(sb, sa, kn, q0, o0, va1, o1, va0, (vb0 = lds128(lds + va + 3072), vb1 = lds128(lds + va + 3584), hook(c, 5)));
        __builtin_amdgcn_s_setprio(0);
        if (c < 7) { ka += 16384; va += vstep; }
        gap(c, 6, settle_a);
        UNIT(sa, sb, kn, q1, o1, va1, o0, vb0, (kc = lds128(lds + ka), hook(c, 6)));
        gap(c, 7, settle_b);
        UNIT(sb, sa, kc, q0, o0, vb1, o1, vb0, (va0 = lds128(lds + va), va1 = lds128(lds + va + 512), hook(c, 7)));
    }
    asm volatile("s_nop 1\n\tv_mfma_f32_32x32x16_bf16 %0, %2, %3, %0\n\ts_nop 7\n\ts_nop 7" : "+v"(o1), "+v"(sa) : "v"(vb1), "v"(p1));
    asm volatile("" : "+v"(o0));
}

__device__ __forceinline__ void pexp(const f32x16& s, float m, bf16x8& p0, bf16x8& p1) {
    float e[16];
#pragma unroll
    for (int i = 0; i < 16; ++i) e[i] = __builtin_amdgcn_exp2f(s[i] - m);
    p0 = __builtin_bit_cast(bf16x8, (u32x4){pk(e[0], e[1]), pk(e[2], e[3]), pk(e[4], e[5]), pk(e[6], e[7])});
    p1 = __builtin_bit_cast(bf16x8, (u32x4){pk(e[8], e[9]), pk(e[10], e[11]), pk(e[12], e[13]), pk(e[14], e[15])});
}

__global__ __launch_bounds__(512, 2) void fused(const char* __restrict__ x, const char* __restrict__ wqkv, const char* __restrict__ wo,
                                                const char* __restrict__ wg, const char* __restrict__ wu, const char* __restrict__ wd,
                                                char* __restrict__ out, char* ws_) {
    extern __shared__ __attribute__((aligned(16))) char smem[];
    lptr lds = (lptr)smem;
    const unsigned lb = (unsigned)(size_t)lds;
    STAMP_DECL; STAMP(0);
    const int tid = threadIdx.x, lane = tid & 63, w = rfl(tid >> 6);
    const int b = blockIdx.x, xcd = b & 7, li = b >> 3;
    const int r = lane & 31, h = lane >> 5;
    const unsigned l16 = lane * 16;
    GAS u64* fl = (GAS u64*)(ws_ + WS_FL);
    const rsrc_t ws = __builtin_amdgcn_make_buffer_rsrc(ws_, 0, WS_END, 0x00020000);
    asm volatile("" :: "s"(x), "s"(wqkv), "s"(wo), "s"(wg), "s"(wu), "s"(wd), "s"(out), "s"(ws_));
    const int rb1 = (xcd >> 1) * 8 + (li >> 2), hq = (xcd & 1) * 4 + (li & 3);
    const int head = xcd * 4 + (li & 3), qb = li >> 2;
    const int rb = xcd * 4 + (li >> 3), cb = li & 7;
    const int g4 = (xcd >> 2) * 8 + (li >> 2), j4 = (xcd & 3) * 4 + (li & 3);
    const int start1 = ((li >> 2) + (li & 3) * 2 + xcd) & 7;
    const unsigned woff = (8 * (w & 3) + (lane >> 3)) * 3072 + (((lane & 7) ^ ((lane >> 2) & 4)) << 4);
    const char* wsrc = wqkv + hq * 128;
    auto issue = [&](int s) {
        glds3(wsrc + (size_t)((s + 2 * start1) & 15) * 32 * 3072, woff, lb + P1_W + (s & 7) * 12288 + (w & 3) * 3072);
    };
    const int qp = w & 3, kh = w >> 2;
    unsigned ka0 = kh * 4096 + l16;
    unsigned va0 = r < 16 ? 8192 + kh * 4096 + h * 256 + r * 16 : M_CONST + (((r - 20) & 31) < 8 ? 64 : 0) + (((r & 27) == 16) ? 0 : 16);
    unsigned vstep = r < 16 ? 16384 : 0;
    unsigned xa1 = (32 * (w & 1) + r) * 128 + ((h ^ ((r >> 1) & 7)) << 4), wa1[2];
    {
        const int q = (lane & 15) >> 2, p = lane & 3, blk = (lane >> 4) & 1;
#pragma unroll
        for (int i = 0; i < 2; ++i) wa1[i] = P1_W + h * 3072 + q * 128 + (((i * 4 + blk * 2 + (p >> 1)) ^ (((q >> 1) & 1) << 2)) << 4) + (p & 1) * 8;
    }
    f32x16 acc1[3] = {};
    unsigned tlbw = 0;
    const char* pc; int cl0, cl1, cmid;
    auto issue_l = [&](int s) {
        glds3_upper(w, wsrc + (size_t)((s + 2 * start1) & 15) * 32 * 3072, woff, lb + P1_W + (s & 7) * 12288 + (w & 3) * 3072);
    };
    p1_norm(lds, x + (size_t)rb1 * 65536, lane, w, (li & 3) * 2, [&](int c) __attribute__((always_inline)) {
        if (c < 0) {
            asm volatile("s_getpc_b64 %0\n.Lpf_here:\n\ts_mov_b32 %1, .Lpf_end0-.Lpf_here\n\ts_mov_b32 %2, .Lpf_end1-.Lpf_here\n\ts_mov_b32 %3, .Lpf_mid-.Lpf_here"
                         : "=s"(pc), "=s"(cl0), "=s"(cl1), "=s"(cmid));
            const int ix = li * 8 + w;
            touch(tlbw, pc + (ix < 128 ? ix * 64 : cmid + (ix - 128) * 64), ix, 0, 192);
            issue_l(0); issue_l(1);
            asm volatile("" : "+v"(ka0), "+v"(va0), "+v"(vstep), "+v"(xa1), "+v"(wa1[0]), "+v"(wa1[1]), "+v"(acc1[0]), "+v"(acc1[1]), "+v"(acc1[2]));
        } else if (c == 7) issue_l(2);
    });
    STAMP(7); STAMPM(1, 1);
    asm volatile("s_waitcnt vmcnt(3) lgkmcnt(0)\n\ts_barrier" : "+s"(tlbw) :: "memory");
    u64 e_raw = __hip_atomic_load(fl + FL_E, RLX);
    const u32x4 poison = {POISON, POISON, POISON, POISON};
    const char* ksrc = ws_ + WS_K + (size_t)head * 65536;

    GAS u64* myf2 = fl + FL_2 + (qb * 8 + qp * 2 + kh) * 32 + head;
    auto attn_finish = [&](f32x16& o0, f32x16& o1, bf16x8 q0, bf16x8 q1, u64 tag, auto reload_kv, auto reload_w) __attribute__((always_inline)) {
    STAMP(5); STAMPM(4, 15);
    const int lane = fresh_lane();
    {
        LAS float* xo = (LAS float*)(lds + M_X + w * 2304);
        LAS float* xi = (LAS float*)(lds + M_X + (w ^ 4) * 2304);
        const unsigned odst = WS_O + ((qb * 8 + qp * 2 + kh) * 32 + head) * 1024;
        float mag = 0.f;
#pragma unroll
        for (int i = 0; i < 8; ++i) mag = fmaf(fabsf(o0[i]), 0.0625f, fmaf(fabsf(o1[i]), 0.0625f, mag));
        const bool ok = (mag <= 5.0e36f) & (o0[8] >= 0x1p-31f) & (o0[8] <= 0x1p99f) & (o1[8] >= 0x1p-31f) & (o1[8] <= 0x1p99f);
        if (!ok) *(LAS unsigned*)(lds + M_WORD) = 1u;
#pragma unroll
        for (int i = 0; i < 9; ++i) xo[i * 64 + lane] = kh ? o0[i] : o1[i];
        VMCLR();
        STAMPM(2, 1);
        LBAR();
        STAMPM(2, 2);
        float o[9];
#pragma unroll
        for (int i = 0; i < 9; ++i) o[i] = (kh ? o1[i] : o0[i]) + xi[i * 64 + lane];
        if (rfl(*(LAS unsigned*)(lds + M_WORD))) {
            LBAR();
            reload_kv();
            WAITBAR(0);
            const f32x16 zero = {};
            float m0 = -INFINITY, m1 = -INFINITY;
            unsigned ka = ka0, va = va0;
#pragma unroll 1
            for (int i = 0; i < 32; ++i) {
                const bf16x8 kf = lds128(lds + ka);
                const f32x16 s0 = MFMA(kf, q0, zero), s1 = MFMA(kf, q1, zero);
#pragma unroll
                for (int j = 0; j < 16; ++j) { m0 = fmaxf(m0, s0[j]); m1 = fmaxf(m1, s1[j]); }
                ka += ((i & 3) == 3) ? 13312 : 1024;
            }
            m0 = fmaxf(m0, __shfl_xor(m0, 32)); m1 = fmaxf(m1, __shfl_xor(m1, 32));
            xo[lane] = m0; xo[64 + lane] = m1;
            LBAR();
            m0 = fmaxf(m0, xi[lane]); m1 = fmaxf(m1, xi[64 + lane]);
            LBAR();
            o0 = zero; o1 = zero; ka = ka0;
#pragma unroll 1
            for (int i = 0; i < 32; ++i) {
                const unsigned vo = va + (i & 3) * 1024;
                const bf16x8 kf = lds128(lds + ka), v0 = lds128(lds + vo), v1 = lds128(lds + vo + 512);
                bf16x8 pa, pb;
                pexp(MFMA(kf, q0, zero), m0, pa, pb); o0 = MFMA(v0, pa, o0); o0 = MFMA(v1, pb, o0);
                SBAR();
                pexp(MFMA(kf, q1, zero), m1, pa, pb); o1 = MFMA(v0, pa, o1); o1 = MFMA(v1, pb, o1);
                ka += ((i & 3) == 3) ? 13312 : 1024; if ((i & 3) == 3) va += vstep;
            }
#pragma unroll
            for (int i = 0; i < 9; ++i) xo[i * 64 + lane] = kh ? o0[i] : o1[i];
            LBAR();
#pragma unroll
            for (int i = 0; i < 9; ++i) o[i] = (kh ? o1[i] : o0[i]) + xi[i * 64 + lane];
            reload_w();
            VMWAIT(0);
        }
        const float inv = __builtin_amdgcn_rcpf(o[8]);
        st16(ws, l16, odst, (u32x4){pk(o[0] * inv, o[1] * inv), pk(o[2] * inv, o[3] * inv), pk(o[4] * inv, o[5] * inv), pk(o[6] * inv, o[7] * inv)});
    }
    LBAR();
    STAMP(6); STAMPM(2, 3);
    };
    auto put_tile = [&](lptr base, int lane_, int q, const f32x16& c) __attribute__((always_inline)) {
#pragma unroll
        for (int g = 0; g < 4; ++g)
            *(LAS f32x4*)(base + g * 1024 + ((lane_ ^ (2 * ((q >> 1) * 4 + g))) << 4)) = (f32x4){c[4 * g], c[4 * g + 1], c[4 * g + 2], c[4 * g + 3]};
    };
    auto final_sum = [&]() __attribute__((always_inline)) {
        const int ln = fresh_lane(), cg = ln & 7, rr = 8 * (w & 3) + (ln >> 3), q = (cg >> 2) * 2 + (w >> 2);
        const unsigned so = (cg & 3) * 1024 + ((rr ^ (2 * cg)) << 4);
        lptr pl = lds + opaque(S1 + q * 4096 + so);
        lptr pc = lds + opaque(((q & 2) ? 0 : S3) + (q & 1) * 16384 + so);
        LBAR();
        STAMPM(3, 15);
        f32x4 a0 = *(LAS f32x4*)pl, a1 = *(LAS f32x4*)(pl + 512);
#pragma unroll
        for (int s = 1; s < 8; ++s) {
            lptr p = s < 4 ? pl + s * 16384 : pc + (s - 4) * 4096;
            a0 += *(LAS f32x4*)p; a1 += *(LAS f32x4*)(p + 512);
        }
        const u32x4 o = {pk(a0[0], a0[1]), pk(a0[2], a0[3]), pk(a1[0], a1[1]), pk(a1[2], a1[3])};
        char* p = out + (size_t)(rb * 64 + 8 * w + (ln >> 3)) * 1024 + cb * 128 + cg * 16;
        *(u32x4*)p = o;
    };
    if (w >= 4) {
        f32x16 o0 = {}, o1 = {};
        bf16x8 q0, q1;
        u32x4 stash[32];
        u64 tag;
        f32x4 csA, csB;
        const int l = w - 4;
        u32x4 jr[2][4];
        asm volatile(".Lpf_mid:");
        const int clen = (cl0 > cl1 ? cl0 : cl1) + COLD_BYTES, iy = li * 4 + l;
        auto code = [&](int k) { touch(tlbw, pc + (iy + 128 * k) * 64, (iy + 128 * k) * 64 + 4, 0, clen); };
        STAMPM(1, 2);
        issue(3); issue(4); issue(5); issue(6); issue(7); STAMPM(1, 3);
        VMWAIT(12); BAR(); STAMPM(1, 4);
        issue(8); issue(9); code(1); code(2); STAMPM(1, 5);
        VMWAIT(12); BAR(); STAMPM(1, 6);
        issue(10); issue(11); code(3); code(4);
        VMWAIT(12); BAR(); STAMPM(1, 7);
        issue(12); issue(13); code(5);
        touch(tlbw, wg + (size_t)iy * 65536, iy, 0, 28); touch(tlbw, wu + (size_t)(iy - 28) * 65536, iy, 28, 28);
        touch(tlbw, wd + (size_t)(iy - 56) * 65536, iy, 56, 28); touch(tlbw, wo + (size_t)(iy - 84) * 65536, iy, 84, 8);
        VMWAIT(12); BAR(); STAMPM(1, 8);
        issue(14); issue(15); STAMPM(1, 9);
        int bj = b;
        asm volatile("" : "+s"(bj));
        const int krow = lane >> 3, ch = lane & 7;
        const bool isg = bj < 112, isd = bj < 224;
        const int j0 = (isd ? bj - 112 : bj - 224) * 8 + 2 * l;
        const int nks = (isd && !isg) ? 112 : 32;
        const int lc = isg ? bj >> 2 : (isd ? j0 / 112 : j0 >> 5), ks0 = isg ? (bj & 3) * 8 + 2 * l : j0 - lc * nks;
        const unsigned rstride = isg ? 3584 : 1024;
        const unsigned jo = (ks0 * 16 + krow) * rstride + lc * 128 + ch * 16;
        const char* jsrc = isg ? wg : (isd ? wd : wo);
        const unsigned jbase = isg ? WS_WGU + lc * 4 * 32768 : (isd ? WS_WD : WS_WO) + lc * 2 * nks * 1024;
        const bool perm = isd && !isg;
        const unsigned jdst0 = jbase + (perm ? (ks0 % 7) * 16 + ks0 / 7 : ks0) * 1024, jdst1 = jbase + (perm ? ((ks0 + 1) % 7) * 16 + (ks0 + 1) / 7 : ks0 + 1) * 1024;
        const unsigned jvo = isg ? l16 : WS_END;
        {
            const rsrc_t ra = __builtin_amdgcn_make_buffer_rsrc((void*)jsrc, 0, isd ? 1835008 : 524288, 0x00020000);
            const rsrc_t rb2 = __builtin_amdgcn_make_buffer_rsrc((void*)wu, 0, isg ? 1835008 : 0, 0x00020000);
#pragma unroll
            for (int j = 0; j < 2; ++j) {
                jr[j][0] = __builtin_amdgcn_raw_buffer_load_b128(ra, jo + j * 16 * rstride, 0, 2); jr[j][1] = __builtin_amdgcn_raw_buffer_load_b128(ra, jo + (j * 16 + 8) * rstride, 0, 2);
                jr[j][2] = __builtin_amdgcn_raw_buffer_load_b128(rb2, jo + j * 16 * rstride, 0, 2); jr[j][3] = __builtin_amdgcn_raw_buffer_load_b128(rb2, jo + (j * 16 + 8) * rstride, 0, 2);
            }
        }
        STAMPM(1, 10); STAMPM(5, 1);
        VMWAIT(20); BAR(); STAMPM(1, 11);
        touch(tlbw, ws_ + (size_t)iy * 262144, iy, 0, 96); touch(tlbw, out + (size_t)(iy - 96) * 65536, iy, 96, 32); code(6);
        VMWAIT(14); BAR(); STAMPM(1, 12);
        code(7); code(8); code(9);
        VMWAIT(8); BAR(); STAMPM(1, 13);
        asm volatile("s_waitcnt lgkmcnt(0)" : "+s"(tlbw) :: "memory");
            LBAR();
        STAMP(1); STAMPM(1, 14); STAMPM(5, 2);
        VMCLR();
        STAMPM(5, 3);
        PIN(e_raw);
        tag = ((u64)(unsigned)rfl((int)(e_raw >> 32)) << 32 | (unsigned)rfl((int)e_raw)) + 1;
        if (l == 1) {
            const unsigned v = ((lane & 3) == 0) ? ONES2 : 0u;
            *(LAS u32x4*)(lds + M_CONST + (lane >> 3) * 512 + (lane & 7) * 16) = (u32x4){v, v, v, v};
            if (lane == 0) *(LAS u32x4*)(lds + M_WORD) = (u32x4){0u, 0u, 0u, 0u};
        }
        u32x4 tf[8];
        f32x16 csa = {};
        {
            lptr st = lds + M_STG + l * 2048;
            lptr ta = st + (4 * h + ((lane & 15) >> 2)) * 64 + ((lane >> 4) & 1) * 32 + (lane & 3) * 8;
            if (isg) {
                lptr sw = st + krow * 64 + ((ch >> 1) & 1) * 1024 + (ch & 1) * 16;
#pragma unroll
                for (int i = 0; i < 4; ++i) {
                    const int j = i >> 1, rr = i & 1;
                    if ((ch >> 2) == rr) {
                        *(LAS u32x4*)sw = jr[j][0]; *(LAS u32x4*)(sw + 512) = jr[j][1];
                        *(LAS u32x4*)(sw + 32) = jr[j][2]; *(LAS u32x4*)(sw + 512 + 32) = jr[j][3];
                    }
#pragma unroll
                    for (int k = 0; k < 2; ++k) {
                        const bf16x8 f = cat(ldstr(ta + k * 1024), ldstr(ta + k * 1024 + 512));
                        const unsigned sel = (r == 2 * rr + k) ? ONES2 : 0u;
                        csa = MFMA(BC8(((u32x4){sel, sel, sel, sel})), f, csa);
                        tf[j * 4 + 2 * rr + k] = __builtin_bit_cast(u32x4, f);
                    }
                }
            } else {
                lptr sw = st + krow * 64 + (ch >> 2) * 1024 + (ch & 3) * 16;
#pragma unroll
                for (int j = 0; j < 2; ++j) {
                    *(LAS u32x4*)sw = jr[j][0]; *(LAS u32x4*)(sw + 512) = jr[j][1];
#pragma unroll
                    for (int k = 0; k < 2; ++k) {
                        tf[j * 4 + k] = __builtin_bit_cast(u32x4, cat(ldstr(ta + k * 1024), ldstr(ta + k * 1024 + 512)));
                        tf[j * 4 + 2 + k] = tf[j * 4 + k];
                    }
                }
            }
        }
        const u32x4 csv = {__float_as_uint(csa[0]), __float_as_uint(csa[1]), __float_as_uint(csa[2]), __float_as_uint(csa[3])};
        if (l == 0) {
            STAMPM(5, 4);
            __builtin_amdgcn_s_sleep(SL1);
            STAMPM(5, 5);
            poll(fl + FL_1 + (head >> 2) * 128 + qb * 16, 16, tag, lane);
            STAMP(4); STAMPM(5, 6);
            gld8(ksrc - (WS_K - WS_Q) + qb * 8192, l16, lb + M_Q);
            gld8(ksrc + qb * 8192, l16, lb); gld8(ksrc + (WS_V - WS_K) + qb * 8192, l16, lb + 8192);
        }
        u32x4 cp[8];
        u64 fa, fc;
        STAMPM(5, 7);
        WAITBAR(0);
        STAMP(3); STAMPM(5, 8);
        q0 = lds128(lds + M_Q + qp * 2048 + l16); q1 = lds128(lds + M_Q + qp * 2048 + 1024 + l16);
        const char* gu = ws_ + WS_WGU + (size_t)j4 * 7 * 32768 + l * 8192;
        const unsigned lq = lb + l * 8192;
        unsigned cnt;
        const unsigned sdo = WS_WD + (cb * 2 * 112 + 4 * l) * 1024;
        GAS u64* fra = fl + FL_R + 4 * (112 + 14 * cb) + (lane < 56 ? lane : 55);
        GAS u64* frc = fl + FL_R + 16 * ((7 * j4) >> 2) + (lane < 16 * (((7 * j4 + 6) >> 2) - ((7 * j4) >> 2) + 1) ? lane : 0);
        attn_main(lds, ka0, va0, vstep, q0, q1, o0, o1, [&](int c, int i) __attribute__((always_inline)) {
            const int n = c * 8 + i;
            if (n >= 1 && n < 17 && (n & 1) == 1) {
                const int m = n >> 1, k = m & 3;
                st16(ws, k < 2 ? l16 : jvo, ((m >> 2) ? jdst1 : jdst0) + k * nks * 1024, tf[m]);
            }
            if (n == 16) st16(ws, (isg && h == 0) ? r * 16 : WS_END, WS_CS + (((b & 3) * 4 + l) * 28 + lc) * 512, csv);
            if (n == 30) { fa = __hip_atomic_load(fra, RLX); fc = __hip_atomic_load(frc, RLX); }
            if (n == 33) {
#pragma unroll
                for (int k = 0; k < 4; ++k) {
                    const unsigned so = WS_CS + ((4 * l + k) * 28 + ((7 * j4) >> 2)) * 512;
                    cp[k] = ld16(ws, l16, so); cp[4 + k] = ld16(ws, r * 16, so + 1024);
                }
            }
            if (n == 33 || n == 49) arrive(lb + M_WORD + (n == 33 ? 4 : 8));
            if (n == 35 || n == 51) cnt = *(volatile LAS unsigned*)(lds + M_WORD + (n == 35 ? 4 : 8));
            if (n >= 36 && n < 60) {
                const int m0_ = (n - 36) * 4 / 3, m1_ = (n - 35) * 4 / 3;
#pragma unroll
                for (int m = m0_; m < m1_; ++m) stash[m] = ld16(ws, l16, sdo + ((m & 1) * 112 + (m >> 3) * 16 + ((m >> 1) & 3)) * 1024);
                gld1(gu + ((n - 36) >> 3) * 32768 + ((n - 36) & 7) * 1024, l16, lq + ((n - 36) >> 3) * 32768 + ((n - 36) & 7) * 1024);
            }
        }, [&](int c, int i, auto settle) __attribute__((always_inline)) {
            const int n = c * 8 + i;
            if (n == 2) STAMPM(4, 1); if (n == 6) STAMPM(4, 2); if (n == 7) STAMPM(4, 3); if (n == 14) STAMPM(4, 4); if (n == 15) STAMPM(4, 5); if (n == 20) STAMPM(4, 6); if (n == 21) STAMPM(4, 7);
            if (n == 24) STAMPM(4, 8); if (n == 32) STAMPM(4, 9); if (n == 36) STAMPM(4, 10); if (n == 40) STAMPM(4, 11); if (n == 48) STAMPM(4, 12); if (n == 56) STAMPM(4, 13); if (n == 60) STAMPM(4, 14);
            if (n == 6 || n == 14) {
                BAR();
            } else if (n == 20) {
                settle();
                VMWAIT(0);
                publish(fl + FL_R + b * 4 + l, tag, lane);
            } else if (n == 33) {
                settle();
                PIN(fa); PIN(fc);
                if (!__all(fa == tag && fc == tag)) { RCINC(6, 1); poll(fra, tag); poll(frc, tag); }
            } else if (n == 36 || n == 52) {
                settle();
                if (n == 36) {
                    asm volatile("" : "+v"(cp[0]), "+v"(cp[1]), "+v"(cp[2]), "+v"(cp[3]), "+v"(cp[4]), "+v"(cp[5]), "+v"(cp[6]), "+v"(cp[7]));
                    csA = (__builtin_bit_cast(f32x4, cp[0]) + __builtin_bit_cast(f32x4, cp[1])) + (__builtin_bit_cast(f32x4, cp[2]) + __builtin_bit_cast(f32x4, cp[3]));
                    csB = (__builtin_bit_cast(f32x4, cp[4]) + __builtin_bit_cast(f32x4, cp[5])) + (__builtin_bit_cast(f32x4, cp[6]) + __builtin_bit_cast(f32x4, cp[7]));
                    PIN(csA); PIN(csB);
                }
                if ((unsigned)rfl(cnt) < 512u) { RCINC(1, 1); wait_lds(lds + M_WORD + (n == 36 ? 4 : 8), 512u); }
            }
        });
        attn_finish(o0, o1, q0, q1, tag, []() {}, [&]() { gld8(gu, l16, lq + S0); gld8(gu + 32768, l16, lq + S1); gld8(gu + 65536, l16, lq + S2); });
        const char* wdn = ws_ + WS_WD + (size_t)((cb * 2 + (l >> 1)) * 112 + 80) * 1024 + (l & 1) * 8192;
        {
            __builtin_amdgcn_s_sleep(SL2);
            const char* os = ws_ + WS_O + (size_t)(rb * 2 + (l >> 1)) * 32768 + (l & 1) * 4096;
            const unsigned lo = lb + S3 + l * 4096;
            lptr co = lds + opaque(S3 + l * 4096 + l16);
            gld4(os, l16, lo); gld4(os + 8192, l16, lo + 16384); gld4(os + 16384, l16, lo + 32768); gld4(os + 24576, l16, lo + 49152);
            STAMP(7); STAMPM(2, 4);
            auto stale = [&](int q) {
                return __any(mx(*(LAS u32x4*)(co + q * 16384 + 3072), mx(*(LAS u32x4*)(co + q * 16384 + 2048), mx(*(LAS u32x4*)(co + q * 16384 + 1024),
                             mx(*(LAS u32x4*)(co + q * 16384), 0u)))) == POISON);
            };
            sfor<0, 4>([&](auto q_) __attribute__((always_inline)) {
                constexpr int q = decltype(q_)::v;
                if (q == 0) { VMWAIT(12); publish(myf2, tag, lane); } else if (q == 1) VMWAIT(9); else if (q == 2) VMWAIT(5); else VMWAIT(1);
                if (__builtin_expect(stale(q), 0)) {
#pragma nounroll
                    for (int t = 0;; ++t) {
                        RCINC(2, 1);
                        VMWAIT(0);
                        if (t == 32) poll(fl + FL_2 + (rb * 2 + (l >> 1)) * 32, 32, tag, lane);
#pragma unroll
                        for (int p = q; p < 4; ++p) if (t == 32 || stale(p)) gld4(os + p * 8192, l16, lo + p * 16384);
                        VMWAIT(0);
                        if (t == 32 || !stale(q)) break;
                    }
                }
                LBAR();
                if (q == 0) STAMPM(2, 6);
            });
        }
        if (b == 0 && l == 0) publish(fl + FL_E, tag, lane);
        LBAR();
        STAMPM(2, 10);
        if (l == 0 && lane == 0) *(LAS unsigned*)(lds + M_CNT) = 0u;
        *(LAS f32x4*)(lds + M0 + 1024 + 2048 * l + l16) = csA; *(LAS f32x4*)(lds + M0 + 1024 + 2048 * l + 1024 + l16) = csB;
        STAMPM(2, 12);
        LBAR();
        gld8(gu + 3 * 32768, l16, lq + S3);
        {
            const int ft = ((7 * j4) & 3) + 2 * l + h;
            LAS float* p = (LAS float*)(lds + M0 + 1024 + (ft >> 2) * 512 + r * 16 + (ft & 3) * 4);
            *(LAS float*)(lds + M0 + (l * 64 + lane) * 4) = (p[0] + p[512]) + (p[1024] + p[1536]);
        }
        WAITBAR(0);
        STAMPM(3, 1);
        gld8(gu + 4 * 32768, l16, lq + S0); gld8(gu + 5 * 32768, l16, lq + S1); gld8(gu + 6 * 32768, l16, lq + S2);
        STAMPM(3, 2);
        WAITBAR(16); STAMPM(3, 3); gld8(wdn, l16, lq + S3); STAMPM(3, 4);
        WAITBAR(16); STAMPM(3, 5); gld8(wdn + 16384, l16, lq + S0); STAMPM(3, 6);
        WAITBAR(0); STAMPM(3, 7);
        f32x16 C[4];
        u32x4 af[20][2], wx[8];
        constexpr int AH = 6;
        auto aoff = [&](int k, int t) { return WS_ACT + ((rb * 2 + t) * 112 + (4 * l + (k & 3)) * 7 + (k >> 2)) * 1024; };
#pragma unroll
        for (int k = 0; k < AH; ++k) { af[k][0] = ld16(ws, l16, aoff(k, 0)); af[k][1] = ld16(ws, l16, aoff(k, 1)); }
        SBAR();
#pragma unroll
        for (int k = 0; k < 20; ++k) {
            if (k == 0) STAMPM(3, 9);
            if ((k & 1) == 0) verify4(af[k][0], af[k][1], af[k + 1][0], af[k + 1][1], ws, l16, aoff(k, 0), aoff(k, 1), aoff(k + 1, 0), aoff(k + 1, 1), fl + FL_4 + rb * 32, 32, tag RC(5));
            if (k == 0) STAMPM(3, 10); if (k == 4) STAMPM(3, 11); if (k == 8) STAMPM(3, 12); if (k == 16) STAMPM(3, 13);
#pragma unroll
            for (int q = 0; q < 4; ++q) C[q] = MFMA(BC8(k < 16 ? stash[2 * k + (q >> 1)] : wx[2 * (k - 16) + (q >> 1)]), BC8(af[k][q & 1]), k ? C[q] : f32x16{});
            SBAR();
            if (k + AH < 20) { af[k + AH][0] = ld16(ws, l16, aoff(k + AH, 0)); af[k + AH][1] = ld16(ws, l16, aoff(k + AH, 1)); }
            if (k >= 6 && k < 10) {
                const unsigned wo4 = WS_WD + (cb * 2 * 112 + 64 + 4 * l + k - 6) * 1024;
                wx[2 * (k - 6)] = ld16(ws, l16, wo4); wx[2 * (k - 6) + 1] = ld16(ws, l16, wo4 + 112 * 1024);
            }
            SBAR();
        }
        STAMP(12); STAMPM(3, 14);
        if (l >= 2) {
#pragma nounroll
            for (int t = 0; t < (1 << 20); ++t) {
                if (rfl(__hip_atomic_load((LAS unsigned*)(lds + M_CNT), __ATOMIC_RELAXED, __HIP_MEMORY_SCOPE_WORKGROUP)) == 4) break;
                __builtin_amdgcn_s_sleep(1);
            }
            asm volatile("" ::: "memory");
        }
        {
            lptr pp = lds + opaque(S1 + l * 16384);
            const int ln = fresh_lane();
#pragma unroll
            for (int q = 0; q < 4; ++q) put_tile(pp + q * 4096, ln, q, C[q]);
        }
        STAMP(14);
        final_sum();
        STAMP(15);
        asm volatile(".Lpf_end0:");
    } else {
        f32x16 o0 = {}, o1 = {};
        bf16x8 q0, q1;
        u32x4 stash[32];
        u64 tag;
        if (w >> 1) p1_consume<1>(lds, ws, lane, w & 1, start1, rb1, hq, xa1, wa1, acc1); else p1_consume<0>(lds, ws, lane, w & 1, start1, rb1, hq, xa1, wa1, acc1);
        STAMP(1); STAMPM(1, 14); STAMPM(5, 2);
        LBAR();
        VMWAIT(0);
        PIN(e_raw);
        tag = ((u64)(unsigned)rfl((int)(e_raw >> 32)) << 32 | (unsigned)rfl((int)e_raw)) + 1;
        publish(fl + FL_1 + (hq * 32 + rb1) * 4 + w, tag, lane);
        STAMP(2); STAMPM(5, 3);
        LBAR();
        STAMP(3);
        q0 = lds128(lds + M_Q + qp * 2048 + l16); q1 = lds128(lds + M_Q + qp * 2048 + 1024 + l16);
        const char* dsrc = ksrc + (w >> 1) * (WS_V - WS_K) + (w & 1) * 4096;
        const unsigned ddst = lb + w * 4096;
        const unsigned swo = WS_WO + (cb * 2 + (w >> 1)) * 32768;
        GAS u64* frb = fl + FL_R + 4 * (224 + 4 * cb) + (lane & 15);
        u32x4 f1, fp;
        bool fpok;
        u64 fb;
        attn_main(lds, ka0, va0, vstep, q0, q1, o0, o1, [&](int c, int i) __attribute__((always_inline)) {
            const int n = c * 8 + i;
            if (n == 0) f1 = ld16(ws, l16, WS_FL + (FL_1 + (head >> 2) * 128) * 8);
            if (n >= 2 && n < 9) gld4(dsrc + ((qb + n - 1) & 7) * 8192, l16, ddst + (n - 1) * 16384);
            if (n >= 14 && n < 38 && (n & 1) == 0) {
                const int m = (n - 14) >> 1;
                if (m < 2) st16(ws, l16, WS_O + ((qb * 8 + 2 * w + m) * 32 + head) * 1024, poison);
                else if (m < 4) st16(ws, l16, WS_H + ((rb * 2 + (w & 1)) * 32 + cb * 4 + (w >> 1) * 2 + m - 2) * 1024, poison);
                else if (m == 4) st16(ws, r * 16, WS_HS + ((rb * 2 + (w & 1)) * 8 + cb) * 512, poison);
                else st16(ws, l16, WS_ACT + ((g4 * 4 + w) * 112 + j4 * 7 + m - 5) * 1024, poison);
            }
            if (n == 33 || n == 49) arrive(lb + M_WORD + (n == 33 ? 4 : 8));
            if (n == 37) fb = __hip_atomic_load(frb, RLX);
            if (n >= 40 && n < 61) {
                const int m0_ = (n - 40) * 4 / 3, m1_ = (n - 39) * 4 / 3;
#pragma unroll
                for (int m = m0_; m < m1_; ++m) stash[m] = ld16(ws, l16, swo + m * 1024);
            }
            if (n == 56 || n == 59) fp = ld16(ws, l16, WS_FL + FL_P * 8 + w * 2048 + (n == 59 ? 1024 : 0));
        }, [&](int c, int i, auto settle) __attribute__((always_inline)) {
            const int n = c * 8 + i;
            if (n == 2) STAMPM(4, 1); if (n == 6) STAMPM(4, 2); if (n == 7) STAMPM(4, 3); if (n == 14) STAMPM(4, 4); if (n == 15) STAMPM(4, 5); if (n == 20) STAMPM(4, 6); if (n == 21) STAMPM(4, 7);
            if (n == 24) STAMPM(4, 8); if (n == 32) STAMPM(4, 9); if (n == 36) STAMPM(4, 10); if (n == 40) STAMPM(4, 11); if (n == 48) STAMPM(4, 12); if (n == 56) STAMPM(4, 13); if (n == 60) STAMPM(4, 14);
            if (n == 2) {
                settle();
                PIN(f1);
                if (!__all(tags2(f1, tag))) { RCINC(0, 1); poll128(fl + FL_1 + (head >> 2) * 128, tag, lane); }
            } else if (n == 6) { VMWAIT(12); BAR(); }
            else if (n == 14) { VMWAIT(0); BAR(); }
            else if (n == 40) {
                settle();
                VMCLR();
                publish(fl + FL_P + b * 4 + w, tag, lane);
                if (!__all(fb == tag)) { RCINC(6, 1); poll(frb, tag); }
            } else if (n == 59) {
                PIN(fp);
                fpok = __all(tags2(fp, tag));
            } else if (n == 62) {
                settle();
                PIN(fp);
                if (!(fpok && __all(tags2(fp, tag)))) { RCINC(7, 1); poll128(fl + FL_P + w * 256, tag, lane); poll128(fl + FL_P + w * 256 + 128, tag, lane); }
            }
        });
        attn_finish(o0, o1, q0, q1, tag, [&]() {
#pragma unroll
            for (int c = 0; c < 6; ++c) gld4(dsrc + ((qb + c) & 7) * 8192, l16, ddst + c * 16384);
        }, []() {});
        {
        const int lane = fresh_lane(), r = lane & 31, h = lane >> 5;
        const int tt = w & 1, ct = w >> 1;
        const int ttile = rb * 2 + tt, ctile = cb * 2 + ct;
        u32x2 rx[4];
        {
            const char* p = x + (size_t)(ttile * 32 + r) * 1024 + (ctile * 32 + 4 * h) * 2;
#pragma unroll
            for (int g = 0; g < 4; ++g) rx[g] = *(const u32x2*)(p + g * 16);
        }
        u32x4 hr[2];
        {
            f32x16 acc = {};
            lptr ba = lds + opaque(S3 + tt * 8192 + l16);
#pragma unroll
            for (int m = 28; m < 32; ++m) stash[m] = ld16(ws, l16, swo + m * 1024);
            VMWAIT(8);
            publish(myf2, tag, lane);
            STAMPM(2, 4);
#pragma unroll
            for (int qd = 0; qd < 4; ++qd) {
                LBAR();
                STAMPM(2, 5 + qd);
                if (qd == 0) STAMP(8);
                u32x4 bf[8];
#pragma unroll
                for (int ks = 0; ks < 8; ++ks) bf[ks] = *(LAS u32x4*)(ba + qd * 16384 + ks * 1024);
                SBAR();
#pragma unroll
                for (int ks = 0; ks < 8; ++ks) acc = MFMA(BC8(stash[qd * 8 + ks]), BC8(bf[ks]), acc);
            }
#pragma unroll
            for (int g = 0; g < 4; ++g) {
                hr[g >> 1][(g & 1) * 2] = pk(acc[4 * g] + bflo(rx[g][0]), acc[4 * g + 1] + bfhi(rx[g][0]));
                hr[g >> 1][(g & 1) * 2 + 1] = pk(acc[4 * g + 2] + bflo(rx[g][1]), acc[4 * g + 3] + bfhi(rx[g][1]));
            }
            st16(ws, l16, WS_H + (ttile * 32 + ctile * 2) * 1024, hr[0]);
            st16(ws, l16, WS_H + (ttile * 32 + ctile * 2 + 1) * 1024, hr[1]);
            float s1 = 0.f, s2 = 0.f;
            stat2(hr[0], s1, s2); stat2(hr[1], s1, s2);
            s1 = halfsum(s1); s2 = halfsum(s2);
            if (h == 0) __hip_atomic_store((GAS u64*)(ws_ + WS_HS) + ((ttile * 8 + cb) * 32 + r) * 2 + ct, (u64)__float_as_uint(s1) | ((u64)__float_as_uint(s2) << 32), RLX);
        }
        STAMP(9); STAMPM(2, 9);
        const int t4 = g4 * 4 + w;
        u32x4 hs[4], hf[32];
        constexpr int HAHEAD = 12;
        const unsigned ho = WS_H + t4 * 32768;
        __builtin_amdgcn_s_sleep(SL3);
#pragma unroll
        for (int i = 0; i < 4; ++i) hs[i] = ld16(ws, l16 + i * 1024, WS_HS + t4 * 4096);
#pragma unroll
        for (int ks = 0; ks < HAHEAD; ++ks) hf[ks] = ld16(ws, l16, ho + ks * 1024);
        LBAR();
        STAMPM(2, 11);
        VMWAIT(4 + HAHEAD);
        STAMPM(2, 12);
        publish(fl + FL_3 + ttile * 16 + ctile, tag, lane);
        if (__builtin_expect(__any(mx(hs[3], mx(hs[2], mx(hs[1], mx(hs[0], 0u)))) == POISON), 0)) {
#pragma nounroll
            for (int t = 0;; ++t) {
                RCINC(3, 1);
                if (t == 32) poll(fl + FL_3 + t4 * 16, 16, tag, lane);
#pragma unroll
                for (int i = 0; i < 4; ++i) hs[i] = ld16(ws, l16 + i * 1024, WS_HS + t4 * 4096);
                VMCLR();
                if (t == 32 || !__any(mx(hs[3], mx(hs[2], mx(hs[1], mx(hs[0], 0u)))) == POISON)) break;
            }
#pragma unroll
            for (int ks = 0; ks < HAHEAD; ++ks) hf[ks] = ld16(ws, l16, ho + ks * 1024);
        }
        LBAR();
        STAMP(10); STAMPM(2, 14);
        float s1 = 0.f, s2 = 0.f;
#pragma unroll
        for (int i = 0; i < 4; ++i) { s1 += __uint_as_float(hs[i][0]) + __uint_as_float(hs[i][2]); s2 += __uint_as_float(hs[i][1]) + __uint_as_float(hs[i][3]); }
        s1 = halfsum(s1); s2 = halfsum(s2);
        const float m = s1 * (1.f / D);
        const float vv = fmaxf(s2 * (1.f / D) - m * m, 0.f) + 1e-5f;
        const float rs = __builtin_amdgcn_rsqf(vv);
        constexpr float C1 = -2.f * 0.7978845608028654f * 1.4426950408889634f, C2 = C1 * 0.044715f;
        const float k1 = C1 * rs, k2 = C2 * rs * rs * rs;
        SBAR();
        f32x16 A[4];
        lptr wlo = lds + opaque(l16), whi = lds + opaque(S2 + l16);
        {
            bf16x8 wf[2][3];
            wf[0][0] = lds128(wlo); wf[0][1] = lds128(wlo + S1); wf[0][2] = lds128(whi);
            SBAR();
#pragma unroll
            for (int ks = 0; ks < 32; ++ks) {
                const int c = ks & 1, n = c ^ 1;
                if ((ks & 3) == 0) verify4(hf[ks], hf[ks + 1], hf[ks + 2], hf[ks + 3], ws, l16, ho + ks * 1024, ho + (ks + 1) * 1024, ho + (ks + 2) * 1024, ho + (ks + 3) * 1024, fl + FL_3 + t4 * 16, 16, tag RC(4));
                if (ks + HAHEAD < 32) hf[ks + HAHEAD] = ld16(ws, l16, ho + (ks + HAHEAD) * 1024);
                if (ks < 31) wf[n][0] = lds128(wlo + (ks + 1) * 1024);
                SBAR(); A[0] = MFMA(wf[c][0], BC8(hf[ks]), ks ? A[0] : f32x16{}); SBAR();
                if (ks < 31) wf[n][1] = lds128(wlo + S1 + (ks + 1) * 1024);
                SBAR(); A[1] = MFMA(wf[c][1], BC8(hf[ks]), ks ? A[1] : f32x16{}); SBAR();
                if (ks < 31) wf[n][2] = lds128(whi + (ks + 1) * 1024);
                SBAR(); A[2] = MFMA(wf[c][2], BC8(hf[ks]), ks ? A[2] : f32x16{}); SBAR();
            }
        }
        LAS float* cs = (LAS float*)(lds + opaque(M0 + 16 * h));
        const unsigned adst = WS_ACT + (t4 * 112 + j4 * 7) * 1024;
        float ea[4], eu[4], et[4];
        f32x4 cg, cu;
        u32x4 eo;
        auto epi = [&](auto G_) __attribute__((always_inline)) {
            constexpr int G = decltype(G_)::v;
            constexpr int hf_ = G / 13, st = G % 13, t = hf_ >> 1, e0 = (hf_ & 1) * 4;

            if constexpr (st == 0) {
#pragma unroll
                for (int e = 0; e < 4; ++e) { ea[e] = fmaf(-m, cg[e], A[t & 3][e0 + e]); PIN(ea[e]); }
            } else if constexpr (st == 1) {
#pragma unroll
                for (int e = 0; e < 4; ++e) { eu[e] = fmaf(-m, cu[e], A[t & 3][8 + e0 + e]); PIN(eu[e]); }
            } else if constexpr (st == 2) {
#pragma unroll
                for (int e = 0; e < 4; ++e) { et[e] = ea[e] * ea[e]; PIN(et[e]); }
            } else if constexpr (st == 3) {
#pragma unroll
                for (int e = 0; e < 4; ++e) { et[e] = fmaf(k2, et[e], k1); PIN(et[e]); }
            } else if constexpr (st == 4) {
#pragma unroll
                for (int e = 0; e < 4; ++e) { et[e] *= ea[e]; PIN(et[e]); }
            } else if constexpr (st == 5) {
#pragma unroll
                for (int e = 0; e < 4; ++e) { eu[e] *= ea[e]; PIN(eu[e]); }
            } else if constexpr (st == 6 || st == 7) {
#pragma unroll
                for (int e = 2 * (st - 6); e < 2 * (st - 6) + 2; ++e) { et[e] = __builtin_amdgcn_exp2f(et[e]); PIN(et[e]); }
                if constexpr (hf_ + 1 < 14) {
                    LAS float* c = cs + ((hf_ + 1) >> 1) * 32 + ((hf_ + 1) & 1) * 8;
                    if (st == 6) cg = *(LAS f32x4*)c; else cu = *(LAS f32x4*)(c + 16);
                }
            } else if constexpr (st == 8) {
#pragma unroll
                for (int e = 0; e < 4; ++e) { et[e] = fmaf(et[e], vv, vv); PIN(et[e]); }
            } else if constexpr (st == 9 || st == 10) {
#pragma unroll
                for (int e = 2 * (st - 9); e < 2 * (st - 9) + 2; ++e) { et[e] = __builtin_amdgcn_rcpf(et[e]); PIN(et[e]); }
            } else if constexpr (st == 11) {
#pragma unroll
                for (int e = 0; e < 4; ++e) { eu[e] *= et[e]; PIN(eu[e]); }
            } else {
                eo[e0 >> 1] = pk(eu[0], eu[1]); eo[(e0 >> 1) + 1] = pk(eu[2], eu[3]);
                if (e0) st16(ws, l16, adst + t * 1024, eo); else { PIN(eo[0]); PIN(eo[1]); }
            }
        };
        auto ftile = [&](auto t_, lptr p) __attribute__((always_inline)) {
            constexpr int t = decltype(t_)::v;
            bf16x8 wf[4];
            wf[0] = lds128(p); wf[1] = lds128(p + 1024); wf[2] = lds128(p + 2048);
            SBAR();
            sfor<0, 32>([&](auto ks_) __attribute__((always_inline)) {
                constexpr int ks = decltype(ks_)::v;
                if (ks + 3 < 32) wf[(ks + 3) & 3] = lds128(p + (ks + 3) * 1024);
                epi(IC<(t - 3) * 32 + ks>{});
                SBAR();
                A[t & 3] = MFMA(wf[ks & 3], BC8(hf[ks]), ks ? A[t & 3] : f32x16{});
                SBAR();
            });
        };
        STAMPM(2, 15);
        LBAR();
        cg = *(LAS f32x4*)cs; cu = *(LAS f32x4*)(cs + 16);
        STAMP(11); STAMPM(3, 1);
        ftile(IC<3>{}, whi + 32768);
        STAMPM(3, 2);
        LBAR();
        STAMPM(3, 3);
        ftile(IC<4>{}, wlo);
        STAMPM(3, 4);
        LBAR();
        STAMPM(3, 5);
        ftile(IC<5>{}, wlo + S1);
        STAMPM(3, 6);
        LBAR();
        STAMPM(3, 7);
        ftile(IC<6>{}, whi);
        STAMPM(3, 8);
        asm volatile("s_waitcnt lgkmcnt(0)" ::: "memory");
        if (lane == 0) __hip_atomic_fetch_add((LAS unsigned*)(lds + M_CNT), 1u, __ATOMIC_RELAXED, __HIP_MEMORY_SCOPE_WORKGROUP);
        sfor<128, 14 * 13>(epi);
        STAMPM(3, 9);
        STAMP(12);
        f32x16 C[4];
        {
            u32x4 af[8][2];
            __builtin_amdgcn_s_sleep(SL4);
            auto aoff = [&](int k, int t) { return WS_ACT + ((rb * 2 + t) * 112 + (4 * w + (k & 3)) * 7 + 5 + (k >> 2)) * 1024; };
#pragma unroll
            for (int k = 0; k < 8; ++k) { af[k][0] = ld16(ws, l16, aoff(k, 0)); af[k][1] = ld16(ws, l16, aoff(k, 1)); }
            lptr wa0 = lds + opaque(w * 4096 + l16), wa3 = lds + opaque(S3 + w * 4096 + l16);
            bf16x8 wf[8][2];
#pragma unroll
            for (int k = 0; k < 8; ++k) { lptr p = (k < 4 ? wa3 : wa0) + (k & 3) * 1024; wf[k][0] = lds128(p); wf[k][1] = lds128(p + 16384); }
            STAMPM(3, 10);
            VMWAIT(16);
            STAMPM(3, 11);
            publish(fl + FL_4 + t4 * 16 + j4, tag, lane);
            STAMP(13); STAMPM(3, 12);
#pragma unroll
            for (int k = 0; k < 8; ++k) {
                if ((k & 1) == 0) verify4(af[k][0], af[k][1], af[k + 1][0], af[k + 1][1], ws, l16, aoff(k, 0), aoff(k, 1), aoff(k + 1, 0), aoff(k + 1, 1), fl + FL_4 + rb * 32, 32, tag RC(5));
#pragma unroll
                for (int q = 0; q < 4; ++q) C[q] = MFMA(wf[k][q >> 1], BC8(af[k][q & 1]), k ? C[q] : f32x16{});
                SBAR();
            }
            STAMPM(3, 13);
            float hv[16];
#pragma unroll
            for (int i = 0; i < 8; ++i) { hv[2 * i] = bflo(hr[i >> 2][i & 3]); hv[2 * i + 1] = bfhi(hr[i >> 2][i & 3]); }
            lptr p3 = lds + opaque(S3 + w * 4096), p0 = lds + opaque(w * 4096);
#pragma unroll
            for (int q = 0; q < 4; ++q) {
                if (q == w) {
#pragma unroll
                    for (int i = 0; i < 16; ++i) C[q][i] += hv[i];
                }
                put_tile(((q & 2) ? p0 : p3) + (q & 1) * 16384, lane, q, C[q]);
            }
        }
        STAMP(14); STAMPM(3, 14);
        final_sum();
        STAMP(15);
        asm volatile(".Lpf_end1:");
        }
    }
    STAMP_FLUSH(ws_);
}

extern "C" void kernel_launch(void* const* d_in, const int* in_sizes, int n_in, void* d_out, int out_size, void* d_ws, size_t ws_size,
                              hipStream_t stream) {
    (void)hipFuncSetAttribute((const void*)fused, hipFuncAttributeMaxDynamicSharedMemorySize, LDS_BYTES);
    fused<<<256, 512, LDS_BYTES, stream>>>((const char*)d_in[0], (const char*)d_in[1], (const char*)d_in[2], (const char*)d_in[4],
                                           (const char*)d_in[5], (const char*)d_in[6], (char*)d_out, (char*)d_ws);
}
```
